```python
import torch

def _softmax(x, axis=-1):
    return torch.softmax(x, dim=axis)
def _gelu(x):
    return torch.nn.functional.gelu(x, approximate="tanh")
def _silu(x):
    return torch.nn.functional.silu(x)
def _sigmoid(x):
    return torch.sigmoid(x)
def _layernorm(x, axis=-1, eps=1e-5):
    m = x.mean(dim=axis, keepdim=True)
    v = x.var(dim=axis, keepdim=True, unbiased=False)
    return (x - m) / torch.sqrt(v + eps)
def _scatter_add(tgt, idx, src, axis=0):
    return tgt.clone().index_add_(axis, idx, src)
def _segment_sum(src, seg, n):
    out = torch.zeros((n,) + src.shape[1:], dtype=src.dtype)
    return out.index_add_(0, seg, src)
def _pad(x, pw):
    flat = tuple(v for pair in reversed(pw) for v in pair)
    return torch.nn.functional.pad(x, flat)

kernel_name = 'standalone__window_attn_swin__vision'

def setup_inputs():

    _w = (lambda a: (lambda t: t.to(torch.bfloat16) if t.is_floating_point() else t)(torch.as_tensor(a)))
    H = W = 512
    C = 96
    ws = 8
    nh = 4
    shift = ws // 2
    coords = torch.stack(torch.meshgrid(torch.arange(ws), torch.arange(ws), indexing='ij'), 0).reshape(2, -1)
    rel = coords[:, :, None] - coords[:, None, :]
    rel[0] += ws - 1
    rel[1] += ws - 1
    rel[0] *= 2 * ws - 1
    rp_idx = rel.sum(0).to(torch.int64)
    img_mask = torch.zeros((1, H, W, 1), dtype=float)
    cnt = 0
    for hs in (slice(0, H - ws), slice(H - ws, H - shift), slice(H - shift, H)):
        for vs in (slice(0, W - ws), slice(W - ws, W - shift), slice(W - shift, W)):
            img_mask[:, hs, vs, :] = cnt
            cnt += 1
    mw = img_mask.reshape(1, H // ws, ws, W // ws, ws, 1).permute(0, 1, 3, 2, 4, 5).reshape(-1, ws * ws)
    am = mw[:, :, None] - mw[:, None, :]
    attn_mask = torch.where(am != 0, -100.0, 0.0).to(float)
    return {
        "x": _w((lambda *s: torch.randn(*s, dtype=torch.bfloat16) * 1.6)(1, H, W, C)),
        "wqkv": _w((lambda *s: torch.randn(*s, dtype=torch.bfloat16) * 1.6)(C, 3 * C) * 0.02),
        "wo": _w((lambda *s: torch.randn(*s, dtype=torch.bfloat16) * 0.05)(C, C) * 0.02),
        "rpb": _w((lambda *s: torch.randn(*s, dtype=torch.bfloat16) * 0.4)((2 * ws - 1) * (2 * ws - 1), nh)),
        "rp_idx": _w(rp_idx),
        "attn_mask": _w(attn_mask),
    }

def reference(x, wqkv, wo, rpb, rp_idx, attn_mask):
    B, H, W, C = x.shape
    N = rp_idx.shape[0]
    ws = int(round(N ** 0.5))
    nh = rpb.shape[-1]
    hd = C // nh
    shift = ws // 2
    xs = torch.roll(x, (-shift, -shift), (1, 2))
    nWh = H // ws
    nWw = W // ws
    xw = (lambda x, *d: x.permute(*d))(xs.reshape(B, nWh, ws, nWw, ws, C), 0, 1, 3, 2, 4, 5).reshape(B * nWh * nWw, N, C)
    qkv = (lambda x, *d: x.permute(*d))(torch.einsum('wnc,cd->wnd', xw, wqkv).reshape(-1, N, 3, nh, hd), 2, 0, 3, 1, 4)
    q, k, v = qkv[0], qkv[1], qkv[2]
    attn = torch.einsum('whid,whjd->whij', q, k) * (hd ** -0.5)
    bias = (lambda x, *d: x.permute(*d))(rpb[rp_idx.reshape(-1)].reshape(N, N, nh), 2, 0, 1)
    attn = attn + bias[None, :, :, :]
    attn = (attn.reshape(B, nWh * nWw, nh, N, N) + attn_mask[None, :, None, :, :]).reshape(-1, nh, N, N)
    attn = _softmax(attn, axis=-1)
    out = (lambda x, *d: x.permute(*d))(torch.einsum('whij,whjd->whid', attn, v), 0, 2, 1, 3).reshape(-1, N, C)
    out = torch.einsum('wnc,cd->wnd', out, wo)
    out = (lambda x, *d: x.permute(*d))(out.reshape(B, nWh, nWw, ws, ws, C), 0, 1, 3, 2, 4, 5).reshape(B, H, W, C)
    return torch.roll(out, (shift, shift), (1, 2))
```

```cpp
#include <hip/hip_runtime.h>
#include <hip/hip_bf16.h>

typedef __bf16 bf16x2 __attribute__((ext_vector_type(2)));
typedef __bf16 bf16x8 __attribute__((ext_vector_type(8)));
typedef short s16x4 __attribute__((ext_vector_type(4)));
typedef float f32x2 __attribute__((ext_vector_type(2)));
typedef float f32x4 __attribute__((ext_vector_type(4)));
typedef float f32x16 __attribute__((ext_vector_type(16)));
typedef unsigned u32x4 __attribute__((ext_vector_type(4)));
typedef unsigned u32x2 __attribute__((ext_vector_type(2)));
#define LDS __attribute__((address_space(3)))
#define DI __device__ __forceinline__

#define LD_AUX " nt"
#define ST_AUX 2

constexpr int ROWB = 512 * 192;
constexpr int XBYTES = 512 * ROWB;
constexpr int MBYTES = 4096 * 8192;
constexpr int NR = 8;

constexpr int L_WO = 0;
constexpr int L_A = 18432;
constexpr int L_B = L_A + 65536;
constexpr int L_STAGE = L_A + 32768;
constexpr int L_IDX = L_B + 65536;
constexpr int L_TBL = L_IDX + 8192;
constexpr int L_FLG = L_TBL + 4096;
constexpr int L_TOTAL = L_FLG + 64;

DI unsigned pk(float a, float b) { f32x2 v = {a, b}; return __builtin_bit_cast(unsigned, __builtin_convertvector(v, bf16x2)); }
DI f32x16 mfma32(u32x4 a, u32x4 b, f32x16 c) { return __builtin_amdgcn_mfma_f32_32x32x16_bf16(__builtin_bit_cast(bf16x8, a), __builtin_bit_cast(bf16x8, b), c, 0, 0, 0); }
DI f32x16 mfma32h(u32x2 a, u32x2 b, f32x16 c) { return __builtin_amdgcn_mfma_f32_32x32x8bf16_1k(__builtin_bit_cast(s16x4, a), __builtin_bit_cast(s16x4, b), c, 0, 0, 0); }
DI f32x4 mfma16(u32x4 a, u32x4 b, f32x4 c) { return __builtin_amdgcn_mfma_f32_16x16x32_bf16(__builtin_bit_cast(bf16x8, a), __builtin_bit_cast(bf16x8, b), c, 0, 0, 0); }
DI float mx3(float a, float b, float c) { return __builtin_elementwise_maximum(__builtin_elementwise_maximum(a, b), c); }
DI u32x4 ld128(LDS const char* p) { return *(LDS const u32x4*)p; }
DI u32x2 ld64(LDS const char* p) { return *(LDS const u32x2*)p; }
DI u32x2 ldtr(LDS const char* p) { return __builtin_bit_cast(u32x2, __builtin_amdgcn_ds_read_tr16_b64_v4i16((LDS s16x4*)p)); }
#define DMA(rs, voff, soff, ldsa, aux) asm volatile("s_mov_b32 m0, %0\n\ts_nop 0\n\tbuffer_load_dwordx4 %1, %2, %3 offen" aux " lds" :: "s"(ldsa), "v"(voff), "s"(rs), "s"(soff) : "memory")
#define PIN(x) asm volatile("" : "+v"(x))
#define SBAR() __builtin_amdgcn_sched_barrier(0)
#define WAIT_BAR(N) asm volatile("s_waitcnt vmcnt(" #N ") lgkmcnt(0)\n\ts_barrier" ::: "memory")

__global__ __launch_bounds__(512, 2) void swin_win_attn_kernel(const char* __restrict__ xg, const char* __restrict__ wqkv, const char* __restrict__ wo,
                                                               const unsigned short* __restrict__ rpb, const int* __restrict__ rp_idx,
                                                               const char* __restrict__ maskg, char* __restrict__ outg) {
    __shared__ __attribute__((aligned(16))) char smem_[L_TOTAL];
    LDS char* smem = (LDS char*)smem_;
    const unsigned lds0 = (unsigned)(size_t)smem;
    const int tid = threadIdx.x, lane = tid & 63;
    const int wid = __builtin_amdgcn_readfirstlane(tid >> 6), grp = wid >> 2, head = wid & 3;
    const int c32 = lane & 31, hf = lane >> 5, c16 = lane & 15, g4 = lane >> 4;
    const __amdgpu_buffer_rsrc_t rsX = __builtin_amdgcn_make_buffer_rsrc((void*)xg, 0, XBYTES, 0x00020000);
    const __amdgpu_buffer_rsrc_t rsM = __builtin_amdgcn_make_buffer_rsrc((void*)maskg, 0, MBYTES, 0x00020000);
    const __amdgpu_buffer_rsrc_t rsO = __builtin_amdgcn_make_buffer_rsrc((void*)outg, 0, XBYTES, 0x00020000);
    const __amdgpu_buffer_rsrc_t rsW = __builtin_amdgcn_make_buffer_rsrc((void*)wqkv, 0, 96 * 288 * 2, 0x00020000);
    const __amdgpu_buffer_rsrc_t rsWo = __builtin_amdgcn_make_buffer_rsrc((void*)wo, 0, 96 * 96 * 2, 0x00020000);

    const int gbase = grp ? L_B : L_A;
    const int lx0 = gbase + (grp ? 32768 : 0), lx1 = lx0 + 12288, lm0 = lx0 + 24576;
    const int lx2 = gbase + (grp ? 0 : 32768), lo = lx2 + 12288, lm1 = lx2 + 24576;
    const int sid = blockIdx.x * 2 + grp;

    unsigned xv[3]; bool xw[3];
#pragma unroll
    for (int i = 0; i < 3; i++) {
        const int p = 64 * i + lane, r = p / 12, t = p - 12 * r, c = t ^ ((r >> 2) & 3);
        xv[i] = (r >> 3) * ROWB + (r & 7) * 192 + 16 * c; xw[i] = (r & 7) >= 4;
    }
    const unsigned mv0 = (lane >> 3) * 128 + 16 * ((lane & 7) ^ ((lane >> 4) & 3)), mv1 = (lane >> 3) * 128 + 1024 + 16 * ((lane & 7) ^ (4 + ((lane >> 4) & 3)));
    const unsigned sv = (c16 >> 3) * ROWB + (c16 & 7) * 192 + 16 * g4; const bool sw = (c16 & 7) >= 4;

    auto win_y = [&](int n) { return 8 * (NR - 1 - n) + (sid >> 6); };
    auto win_x = [&](int n) { return (sid + 9 * n) & 63; };
    auto issue_x = [&](int n, int slot) {
        const int wy = win_y(n), wx = win_x(n);
        unsigned so = ((8 * wy + 4 + 2 * head) & 511) * ROWB;
        unsigned v0 = xv[0], v1 = xv[1], v2 = xv[2];
        if (wx == 63) { PIN(v0); PIN(v1); PIN(v2); v0 += xw[0] ? -768 : 508 * 192; v1 += xw[1] ? -768 : 508 * 192; v2 += xw[2] ? -768 : 508 * 192; }
        else so += (8 * wx + 4) * 192;
        if (n >= NR) so = XBYTES;
        const unsigned d = lds0 + slot + head * 3072;
        DMA(rsX, v0, so, d, LD_AUX); DMA(rsX, v1, so, d + 1024, LD_AUX); DMA(rsX, v2, so, d + 2048, LD_AUX);
    };
    auto issue_m = [&](int n, int slot) {
        unsigned so = (win_y(n) * 64 + win_x(n)) * 8192 + head * 2048;
        if (n >= NR) so = MBYTES;
        const unsigned d = lds0 + slot + head * 2048;
        DMA(rsM, mv0, so, d, LD_AUX); DMA(rsM, mv1, so, d + 1024, LD_AUX);
    };

    u32x4 id0, id1; unsigned rb0, rb1;
    {
        const int* ip = rp_idx + 512 * wid + 4 * lane;
        const unsigned short* r0 = rpb + tid; const unsigned short* r1 = rpb + (tid + 512 < 900 ? tid + 512 : 899);
        asm volatile("global_load_dwordx4 %0, %1, off" : "=v"(id0) : "v"(ip));
        asm volatile("global_load_dwordx4 %0, %1, off offset:1024" : "=v"(id1) : "v"(ip));
        asm volatile("global_load_ushort %0, %1, off" : "=v"(rb0) : "v"(r0));
        asm volatile("global_load_ushort %0, %1, off" : "=v"(rb1) : "v"(r1));
    }
    issue_x(0, lx0);
    {
        const unsigned l16 = lane * 16;
#pragma unroll
        for (int i = 0; i < 7; i++) { int p = wid + 8 * i; p = p > 53 ? 53 : p; DMA(rsW, l16, p * 1024, lds0 + L_STAGE + p * 1024, ""); }
#pragma unroll
        for (int i = 0; i < 3; i++) { int p = wid + 8 * i; p = p > 17 ? 17 : p; DMA(rsWo, l16, p * 1024, lds0 + L_WO + p * 1024, ""); }
    }
    issue_x(1, lx1);
    issue_m(0, lm0);
    asm volatile("s_waitcnt vmcnt(18)" : "+v"(id0), "+v"(id1), "+v"(rb0), "+v"(rb1));
    {
        const float rs = 4.898979485566356f;
        *(LDS float*)(smem + L_TBL + (tid & 3) * 1024 + (tid >> 2) * 4) = __uint_as_float(rb0 << 16) * rs;
        if (tid + 512 < 900) *(LDS float*)(smem + L_TBL + (tid & 3) * 1024 + ((tid + 512) >> 2) * 4) = __uint_as_float(rb1 << 16) * rs;
#pragma unroll
        for (int i = 0; i < 2; i++) {
            const u32x4 v = i ? id1 : id0;
            const int e = 512 * wid + 256 * i + 4 * lane, q = e >> 6, j0 = e & 63;
            const int ut = (q >> 5) * 2 + (j0 >> 5), l2 = (q & 31) + 32 * ((j0 >> 2) & 1), r0 = 4 * ((j0 >> 3) & 3);
            u32x2 w = {(v.x * 4) | (v.y * 4) << 16, (v.z * 4) | (v.w * 4) << 16};
            *(LDS u32x2*)(smem + L_IDX + ((ut * 2 + (r0 >> 3)) * 64 + l2) * 16 + (r0 & 7) * 2) = w;
        }
    }
    WAIT_BAR(5);
    u32x4 Wq[6], Wk[6], Wv[6];
    {
        const int q = c16 >> 2, p = lane & 3;
        LDS const char* wb = smem + L_STAGE + (8 * hf + q) * 576 + (24 * head + 16 * (g4 & 1) + 4 * p) * 2;
#pragma unroll
        for (int s = 0; s < 6; s++) {
            u32x2 a, b;
            a = ldtr(wb + 16 * s * 576);       b = ldtr(wb + (16 * s + 4) * 576);       Wq[s] = u32x4{a.x, a.y, b.x, b.y};
            a = ldtr(wb + 16 * s * 576 + 192); b = ldtr(wb + (16 * s + 4) * 576 + 192); Wk[s] = u32x4{a.x, a.y, b.x, b.y};
            a = ldtr(wb + 16 * s * 576 + 384); b = ldtr(wb + (16 * s + 4) * 576 + 384);
            Wv[s] = c32 < 24 ? u32x4{a.x, a.y, b.x, b.y} : u32x4{0, 0, 0, 0};
        }
    }
    u32x4 wof[3];
    {
        const int q = c16 >> 2, p = lane & 3;
#pragma unroll
        for (int i = 0; i < 3; i++) {
            int f = wid + 8 * i; f = f > 17 ? 17 : f;
            const int u = f / 3, s = f - 3 * u, ci = 4 * s + g4;
            const int k0 = ci < 8 ? 24 * (ci >> 1) + 4 * (ci & 1) + q : 24 * (ci - 8) + 16 + q, dk = ci < 8 ? 8 : 4;
            LDS const char* a0 = smem + L_WO + k0 * 192 + (32 * (u >> 1) + 8 * p + 4 * (u & 1)) * 2;
            const u32x2 a = ldtr(a0), b = ldtr(a0 + dk * 192);
            wof[i] = u32x4{a.x, a.y, b.x, b.y};
        }
    }
    f32x16 bias[2][2];
#pragma unroll
    for (int ut = 0; ut < 4; ut++) {
#pragma unroll
        for (int hh = 0; hh < 2; hh++) {
            const u32x4 v = ld128(smem + L_IDX + ((ut * 2 + hh) * 64 + lane) * 16);
#pragma unroll
            for (int k = 0; k < 4; k++) {
                bias[ut >> 1][ut & 1][8 * hh + 2 * k]     = *(LDS const float*)(smem + L_TBL + head * 1024 + (v[k] & 0xffff));
                bias[ut >> 1][ut & 1][8 * hh + 2 * k + 1] = *(LDS const float*)(smem + L_TBL + head * 1024 + (v[k] >> 16));
            }
        }
    }
    WAIT_BAR(5);
#pragma unroll
    for (int i = 0; i < 3; i++) { int f = wid + 8 * i; f = f > 17 ? 17 : f; *(LDS u32x4*)(smem + L_WO + f * 1024 + lane * 16) = wof[i]; }
    issue_x(2, lx2);

    const int rot = (c32 >> 2) & 3;
    const unsigned xaE = c32 * 192 + 16 * (hf ^ rot), xaO = c32 * 192 + 16 * ((2 + hf) ^ rot);
    const int fo_w = (0x78 >> (2 * rot)) & 3, fo_r = (0x78 >> (2 * (c16 >> 2))) & 3;
    const unsigned owA = lo + c32 * 192 + 16 * ((2 * head + hf) ^ fo_w), owB = lo + c32 * 192 + 16 * ((8 + head) ^ fo_w) + 8 * hf;
    const unsigned orA = lo + (16 * head + c16) * 192 + 16 * (g4 ^ fo_r);
    const unsigned ones = c32 < 24 ? 0u : 0x3f803f80u;
    const float cs = (float)(1.4426950408889634 / 4.898979485566356);

    auto out_proj = [&](int n) {
        const int wy = win_y(n), wx = win_x(n);
        unsigned so = ((8 * wy + 4 + 2 * head) & 511) * ROWB, v = sv;
        if (wx == 63) { PIN(v); v += sw ? -768 : 508 * 192; } else so += (8 * wx + 4) * 192;
        u32x4 of[3];
#pragma unroll
        for (int s = 0; s < 3; s++) of[s] = ld128(smem + orA + 64 * s);
#pragma unroll
        for (int u = 0; u < 3; u++) {
            f32x4 a = {}, b = {};
#pragma unroll
            for (int s = 0; s < 3; s++) {
                a = mfma16(ld128(smem + L_WO + (6 * u + s) * 1024 + lane * 16), of[s], a);
                b = mfma16(ld128(smem + L_WO + (6 * u + 3 + s) * 1024 + lane * 16), of[s], b);
            }
            const u32x4 o = {pk(a[0], a[1]), pk(a[2], a[3]), pk(b[0], b[1]), pk(b[2], b[3])};
            __builtin_amdgcn_raw_buffer_store_b128(o, rsO, v + 64 * u, so, ST_AUX);
        }
    };

    if (grp) asm volatile("s_barrier" ::: "memory");
    int xs0 = lx0, xs1 = lx1, xs2 = lx2, ms0 = lm0, ms1 = lm1;
    #pragma unroll 1
    for (int n = 0; n < NR; n++) {
        issue_m(n + 1, ms1);
        if (n > 0) out_proj(n - 1);
        u32x4 Q0[2], K0[2], Vf[4]; u32x2 Q1[2], K1[2];
#pragma unroll
        for (int T = 0; T < 2; T++) {
            f32x16 aq = {}, ak = {}, av = {};
#pragma unroll
            for (int s = 0; s < 6; s++) {
                const u32x4 xf = ld128(smem + xs0 + ((s & 1) ? xaO : xaE) + 64 * (s >> 1) + 6144 * T);
                aq = mfma32(Wq[s], xf, aq); ak = mfma32(Wk[s], xf, ak); av = mfma32(xf, Wv[s], av);
            }
            Q0[T] = u32x4{pk(aq[0], aq[1]), pk(aq[2], aq[3]), pk(aq[4], aq[5]), pk(aq[6], aq[7])}; Q1[T] = u32x2{pk(aq[8], aq[9]), pk(aq[10], aq[11])};
            K0[T] = u32x4{pk(ak[0], ak[1]), pk(ak[2], ak[3]), pk(ak[4], ak[5]), pk(ak[6], ak[7])}; K1[T] = u32x2{pk(ak[8], ak[9]), pk(ak[10], ak[11])};
            Vf[2 * T]     = u32x4{pk(av[0], av[1]) | ones, pk(av[2], av[3]) | ones, pk(av[4], av[5]) | ones, pk(av[6], av[7]) | ones};
            Vf[2 * T + 1] = u32x4{pk(av[8], av[9]) | ones, pk(av[10], av[11]) | ones, pk(av[12], av[13]) | ones, pk(av[14], av[15]) | ones};
            PIN(Q0[T]); PIN(Q1[T]); PIN(K0[T]); PIN(K1[T]); PIN(Vf[2 * T]); PIN(Vf[2 * T + 1]); SBAR();
        }
        if (n == 0) asm volatile("s_waitcnt vmcnt(5)" ::: "memory"); else asm volatile("s_waitcnt vmcnt(8)" ::: "memory");
        {
            const u32x4 z = ld128(smem + ms0 + head * 2048 + lane * 16) | ld128(smem + ms0 + head * 2048 + 1024 + lane * 16);
            const unsigned nz = __builtin_amdgcn_ballot_w64((z.x | z.y | z.z | z.w) != 0) != 0;
            if (lane == 0) *(LDS unsigned*)(smem + L_FLG + wid * 4) = nz;
        }
        asm volatile("s_waitcnt lgkmcnt(0)\n\ts_barrier" ::: "memory");
        issue_x(n + 3, xs0);
        const u32x4 fl = ld128(smem + L_FLG + grp * 16);
        const bool masked = __builtin_amdgcn_readfirstlane(fl.x | fl.y | fl.z | fl.w) != 0;
#pragma unroll
        for (int U = 0; U < 2; U++) {
            f32x16 s0 = mfma32(K0[0], Q0[U], bias[U][0]), s1 = mfma32(K0[1], Q0[U], bias[U][1]);
            s0 = mfma32h(K1[0], Q1[U], s0); s1 = mfma32h(K1[1], Q1[U], s1);
            if (masked) {
                const float rs = 4.898979485566356f;
                int ln = lane; PIN(ln); const int row = 32 * U + (ln & 31), sx = (row >> 1) & 7, hf = ln >> 5;
#pragma unroll
                for (int k = 0; k < 4; k++) {
                    const u32x2 a = ld64(smem + ms0 + row * 128 + 16 * (k ^ sx) + 8 * hf), b = ld64(smem + ms0 + row * 128 + 16 * ((4 + k) ^ sx) + 8 * hf);
                    s0[4 * k]     = __builtin_fmaf(__uint_as_float(a.x << 16), rs, s0[4 * k]);     s0[4 * k + 1] = __builtin_fmaf(__uint_as_float(a.x & 0xffff0000u), rs, s0[4 * k + 1]);
                    s0[4 * k + 2] = __builtin_fmaf(__uint_as_float(a.y << 16), rs, s0[4 * k + 2]); s0[4 * k + 3] = __builtin_fmaf(__uint_as_float(a.y & 0xffff0000u), rs, s0[4 * k + 3]);
                    s1[4 * k]     = __builtin_fmaf(__uint_as_float(b.x << 16), rs, s1[4 * k]);     s1[4 * k + 1] = __builtin_fmaf(__uint_as_float(b.x & 0xffff0000u), rs, s1[4 * k + 1]);
                    s1[4 * k + 2] = __builtin_fmaf(__uint_as_float(b.y << 16), rs, s1[4 * k + 2]); s1[4 * k + 3] = __builtin_fmaf(__uint_as_float(b.y & 0xffff0000u), rs, s1[4 * k + 3]);
                }
            }
            float ma = mx3(s0[0], s0[1], s1[0]), mb = mx3(s0[2], s0[3], s1[1]);
            ma = mx3(ma, s1[2], s1[3]);
#pragma unroll
            for (int r = 4; r < 16; r += 4) { ma = mx3(ma, s0[r], s0[r + 1]); mb = mx3(mb, s0[r + 2], s0[r + 3]); ma = mx3(ma, s1[r], s1[r + 1]); mb = mx3(mb, s1[r + 2], s1[r + 3]); }
            float m = __builtin_elementwise_maximum(ma, mb);
            { auto rr = __builtin_amdgcn_permlane32_swap(__float_as_uint(m), __float_as_uint(m), false, false); m = __builtin_elementwise_maximum(__uint_as_float(rr[0]), __uint_as_float(rr[1])); }
            const float nm = -m * cs;
#pragma unroll
            for (int r = 0; r < 16; r++) { s0[r] = __builtin_amdgcn_exp2f(__builtin_fmaf(s0[r], cs, nm)); s1[r] = __builtin_amdgcn_exp2f(__builtin_fmaf(s1[r], cs, nm)); }
            f32x16 o = {};
            o = mfma32(Vf[0], u32x4{pk(s0[0], s0[1]), pk(s0[2], s0[3]), pk(s0[4], s0[5]), pk(s0[6], s0[7])}, o);
            o = mfma32(Vf[1], u32x4{pk(s0[8], s0[9]), pk(s0[10], s0[11]), pk(s0[12], s0[13]), pk(s0[14], s0[15])}, o);
            o = mfma32(Vf[2], u32x4{pk(s1[0], s1[1]), pk(s1[2], s1[3]), pk(s1[4], s1[5]), pk(s1[6], s1[7])}, o);
            o = mfma32(Vf[3], u32x4{pk(s1[8], s1[9]), pk(s1[10], s1[11]), pk(s1[12], s1[13]), pk(s1[14], s1[15])}, o);
            const float inv = __builtin_amdgcn_rcpf(o[12]);
            *(LDS u32x4*)(smem + owA + 6144 * U) = u32x4{pk(o[0] * inv, o[1] * inv), pk(o[2] * inv, o[3] * inv), pk(o[4] * inv, o[5] * inv), pk(o[6] * inv, o[7] * inv)};
            *(LDS u32x2*)(smem + owB + 6144 * U) = u32x2{pk(o[8] * inv, o[9] * inv), pk(o[10] * inv, o[11] * inv)};
            SBAR();
        }
        asm volatile("s_waitcnt lgkmcnt(0)\n\ts_barrier" ::: "memory");
        { const int t = xs0; xs0 = xs1; xs1 = xs2; xs2 = t; const int u = ms0; ms0 = ms1; ms1 = u; }
    }
    out_proj(NR - 1);
}

extern "C" void kernel_launch(void* const* d_in, const int* in_sizes, int n_in, void* d_out, int out_size, void* d_ws, size_t ws_size, hipStream_t stream) {
    swin_win_attn_kernel<<<256, 512, 0, stream>>>((const char*)d_in[0], (const char*)d_in[1], (const char*)d_in[2], (const unsigned short*)d_in[3],
                                                   (const int*)d_in[4], (const char*)d_in[5], (char*)d_out);
}
```

```cpp
#include <hip/hip_runtime.h>
#include <hip/hip_bf16.h>

typedef __bf16 bf16x2 __attribute__((ext_vector_type(2)));
typedef __bf16 bf16x8 __attribute__((ext_vector_type(8)));
typedef short s16x4 __attribute__((ext_vector_type(4)));
typedef float f32x2 __attribute__((ext_vector_type(2)));
typedef float f32x4 __attribute__((ext_vector_type(4)));
typedef float f32x16 __attribute__((ext_vector_type(16)));
typedef unsigned u32x2 __attribute__((ext_vector_type(2)));
typedef unsigned u32x3 __attribute__((ext_vector_type(3)));
typedef unsigned u32x4 __attribute__((ext_vector_type(4)));
typedef unsigned u32x8 __attribute__((ext_vector_type(8)));
typedef unsigned u32x16 __attribute__((ext_vector_type(16)));
#define LDS __attribute__((address_space(3)))
#define DI __device__ __forceinline__
#define LD_AUX " nt"

constexpr int ROWB = 512 * 192;
constexpr int XBYTES = 512 * ROWB;
constexpr int MBYTES = 4096 * 8192;
constexpr int NR = 8;

constexpr int L_WO = 0;
constexpr int L_CONST = 18432;
constexpr int L_A = 19456;
constexpr int L_B = L_A + 65536;
constexpr int L_STAGE = L_A + 32768;
constexpr int L_IDX = L_STAGE + 55296;
constexpr int L_FLG = L_B + 65536;
constexpr int L_TBL = L_FLG + 2048;
constexpr int L_BAD = L_TBL + 6144;
constexpr int L_TOTAL = L_BAD + 64;

constexpr int swz(int q) { return (0x78 >> (2 * (q & 3))) & 3; }

struct Tables { unsigned wave[8][32]; };
constexpr Tables make_tables() {
    Tables t{};
    for (int w = 0; w < 8; w++) {
        const int grp = w >> 2, head = w & 3;
        const int lx0 = grp ? L_B + 32768 : L_A + 8192, lx1 = lx0 + 12288, lx2 = grp ? L_B + 20480 : L_A + 32768;
        const int lm0 = grp ? L_B + 57344 : L_A, lm1 = grp ? L_B : L_A + 57344, lo = grp ? L_B + 8192 : L_A + 45056;
        const unsigned a[16] = {0, 0, (unsigned)(lx0 + 3072 * head), (unsigned)((grp ? lx1 : lx2) + 3072 * head), 0, (unsigned)((lm0 + 2048 * head) ^ (lm1 + 2048 * head)),
                                (unsigned)(lm0 + 2048 * head), 0, (unsigned)(L_FLG + 1024 * grp + 4 * head), (unsigned)(L_FLG + 1024 * grp), (unsigned)grp, 12288, 0x00ffffffu, 0x00ffffffu,
                                0, 0};
        for (int i = 0; i < 16; i++) t.wave[w][i] = a[i];
        for (int i = 0; i < 3; i++) {
            const int f = w + 8 * i > 17 ? 17 : w + 8 * i, s = f / 6, u = f - 6 * s;
            t.wave[w][16 + i] = 8 * s * 192 + 2 * u; t.wave[w][19 + i] = L_WO + f * 1024;
        }
        t.wave[w][22] = lx0; t.wave[w][23] = lo; t.wave[w][24] = lx2 + 3072 * head; t.wave[w][25] = L_TBL + 1536 * head; t.wave[w][26] = grp * 4096 + head * 128;
    }
    return t;
}
static __device__ const Tables TAB = make_tables();

DI int win0_swap(int r, int c) { return (int)((0x88442211u >> (r & 31)) & (c == 7)) - (int)((0x88442211u >> ((r - 2) & 31)) & (c == 0)); }
DI int win0_y(int r, int c) { return (r + 2 * win0_swap(r, c)) & 63; }
DI int win0_x(int r, int c) { return 56 + c - 7 * win0_swap(r, c); }

DI unsigned pk(float a, float b) { f32x2 v = {a, b}; return __builtin_bit_cast(unsigned, __builtin_convertvector(v, bf16x2)); }
DI f32x16 mfma32(u32x4 a, u32x4 b, f32x16 c) { return __builtin_amdgcn_mfma_f32_32x32x16_bf16(__builtin_bit_cast(bf16x8, a), __builtin_bit_cast(bf16x8, b), c, 0, 0, 0); }
DI f32x16 mfma32h(u32x2 a, u32x2 b, f32x16 c) { return __builtin_amdgcn_mfma_f32_32x32x8bf16_1k(__builtin_bit_cast(s16x4, a), __builtin_bit_cast(s16x4, b), c, 0, 0, 0); }
DI f32x4 mfma16(u32x4 a, u32x4 b, f32x4 c) { return __builtin_amdgcn_mfma_f32_16x16x32_bf16(__builtin_bit_cast(bf16x8, a), __builtin_bit_cast(bf16x8, b), c, 0, 0, 0); }
DI float mx2(float a, float b) { return __builtin_elementwise_maximum(a, b); }
DI u32x4 ld128(LDS const char* p) { return *(LDS const u32x4*)p; }
DI u32x2 ld64(LDS const char* p) { return *(LDS const u32x2*)p; }
DI unsigned scale2(unsigned x, float s) { return pk(__uint_as_float(x << 16) * s, __uint_as_float(x & 0xffff0000u) * s); }
DI u32x4 scale8(u32x4 x, float s) { return u32x4{scale2(x.x, s), scale2(x.y, s), scale2(x.z, s), scale2(x.w, s)}; }
#define DMA(rs, voff, soff, ldsa, aux) asm volatile("s_mov_b32 m0, %0\n\ts_nop 0\n\tbuffer_load_dwordx4 %1, %2, %3 offen" aux " lds" :: "s"(ldsa), "v"(voff), "s"(rs), "s"(soff) : "memory")
#define PIN(x) asm volatile("" : "+v"(x))
#define SBAR() __builtin_amdgcn_sched_barrier(0)
#define WAIT_BAR(N) asm volatile("s_waitcnt vmcnt(" #N ") lgkmcnt(0)\n\ts_barrier" ::: "memory")

__global__ __launch_bounds__(512, 2) void swin_win_attn_kernel(const char* __restrict__ xg, const char* __restrict__ wqkv, const char* __restrict__ wo,
                                                               const unsigned short* __restrict__ rpb, const int* __restrict__ rp_idx,
                                                               const char* __restrict__ maskg, char* __restrict__ outg
) {
    __shared__ __attribute__((aligned(16))) char smem_[L_TOTAL];
    LDS char* smem = (LDS char*)smem_;
    const int tid = threadIdx.x, wid = __builtin_amdgcn_readfirstlane(tid >> 6), bid = blockIdx.x;
    const int lane = tid & 63, grp = wid >> 2, head = wid & 3;
    const unsigned l16 = lane * 16;
    unsigned warm;
    {
        const char* pc; asm volatile("s_getpc_b64 %0" : "=s"(pc));
        const unsigned k = (bid >> 3) * 8 + wid, o = (k < 136 ? k : k - 120) * 128;
        asm volatile("global_load_dword %0, %1, %2" : "=v"(warm) : "v"(o), "s"(pc));
    }
    u32x16 sp, wt;
    {
        const char* wp = (const char*)&TAB.wave[wid][0];
        asm volatile("s_load_dwordx16 %0, %2, 0x0\n\ts_load_dwordx16 %1, %2, 0x40" : "=&{s[48:63]}"(sp), "=&s"(wt) : "s"(wp));
    }
    u32x4 id0, id1; unsigned rb0, rb1;
    {
        const int* ip = rp_idx + 512 * wid;
        asm volatile("global_load_dwordx4 %0, %1, %2" : "=v"(id0) : "v"(l16), "s"(ip));
        asm volatile("global_load_dwordx4 %0, %1, %2 offset:1024" : "=v"(id1) : "v"(l16), "s"(ip));
        const __amdgpu_buffer_rsrc_t rsR = __builtin_amdgcn_make_buffer_rsrc((void*)rpb, 0, 1800, 0x00020000);
        const unsigned l2 = lane * 2, ro = wid * 120;
        asm volatile("buffer_load_ushort %0, %1, %2, %3 offen" : "=v"(rb0) : "v"(l2), "s"(rsR), "s"(ro));
        asm volatile("buffer_load_ushort %0, %1, %2, %3 offen offset:960" : "=v"(rb1) : "v"(l2), "s"(rsR), "s"(ro));
    }
    const __amdgpu_buffer_rsrc_t rsW = __builtin_amdgcn_make_buffer_rsrc((void*)(wqkv + wid * 6912), 0, 6912, 0x00020000);
    const unsigned wld = L_STAGE + wid * 6912, w0 = (bid & 8) ? 3072 : 0;
#pragma unroll
    for (int i = 0; i < 3; i++) DMA(rsW, l16, w0 + i * 1024, wld + w0 + i * 1024, "");

    int ln = lane, bd = bid; PIN(ln); asm volatile("" : "+s"(bd));
    const int srow = (bd & 31) + 32 * grp, scol = (bd >> 5) ^ grp;
    const int wy0 = win0_y(srow, scol), wx0 = win0_x(srow, scol);
    const bool edge = wx0 == 63;
    const int c32 = ln & 31, hf = ln >> 5, c16 = ln & 15, g4 = ln >> 4, q = c16 >> 2, p = ln & 3, fr = swz(c32 >> 2);
    u32x4 t0, tx, t4;
#pragma unroll
    for (int i = 0; i < 3; i++) {
        const int k = 64 * i + ln, r = (k * 171) >> 11, t = k - 12 * r;
        t0[i] = (r >> 3) * ROWB + (r & 7) * 192 + 16 * (t ^ swz(r >> 2)); tx[i] = t0[i] + (!edge ? 0 : (r & 4) ? -768 : 508 * 192);
        t4[i] = wld + (k < 144 ? r * 576 + t * 16 : 11 * 576 + 11 * 16);
    }
    tx[3] = g4 * 192 + 12 * c16;
    t4[3] = l16 ^ (16 * g4);
    const __amdgpu_buffer_rsrc_t rsX = __builtin_amdgcn_make_buffer_rsrc((void*)xg, 0, XBYTES, 0x00020000);
    const __amdgpu_buffer_rsrc_t rsM = __builtin_amdgcn_make_buffer_rsrc((void*)maskg, 0, MBYTES, 0x00020000);
    const __amdgpu_buffer_rsrc_t rsO = __builtin_amdgcn_make_buffer_rsrc((void*)outg, 0, XBYTES, 0x00020000);
    const __amdgpu_buffer_rsrc_t rsWo = __builtin_amdgcn_make_buffer_rsrc((void*)wo, 0, 96 * 96 * 2, 0x00020000);
    const int wy = ln ? (srow + 9 * ln) & 63 : wy0, wx = ln ? (8 * (NR - 1 - ln) + scol) & 63 : wx0;
    const unsigned xo = ((8 * wy + 4 + 2 * head) & 511) * ROWB + (wx == 63 ? 0 : (8 * wx + 4) * 192), mo = (wy * 64 + wx) * 8192 + head * 2048;
    auto x_off = [&](int n) { return (unsigned)__builtin_amdgcn_readlane(xo, n); };
    auto m_off = [&](int n) { return (unsigned)__builtin_amdgcn_readlane(mo, n); };
    asm volatile("s_waitcnt lgkmcnt(0)" : "+{s[48:63]}"(sp), "+s"(wt));
    sp[1] = edge ? 508 * 192 : 0; sp[4] = edge ? 0 : 768;
    u32x16 tb0 = {x_off(0), x_off(1), x_off(2), x_off(3), x_off(4), x_off(5), x_off(6), x_off(7), XBYTES, XBYTES, XBYTES, wt[8], m_off(1), m_off(2), m_off(3), m_off(4)};
    u32x4 tb1 = {m_off(5), m_off(6), m_off(7), MBYTES};
    unsigned mo0 = m_off(0);
    u32x8 ad0 = {wt[6] + c32 * 192 + 16 * (hf ^ fr), wt[6] + c32 * 192 + 16 * ((2 + hf) ^ fr), wt[6] + c16 * 192 + 16 * (g4 ^ swz(q)), wt[7] + c32 * 192 + 16 * (head ^ fr) + 8 * hf,
                 t0.x, t0.y, t0.z, t4.w};
    u32x4 ad1 = {l16, tx.w, wt[7] + (16 * head + 4 * p + q) * 192 + 16 * (g4 ^ swz(p)), 0};
    if (wid == 0) *(LDS u32x4*)(smem + L_CONST + l16) = u32x4{t0.x, t0.y, t0.z, t4.w};
    const unsigned wr = L_STAGE + 48 * head + (8 * hf + q) * 576, wol = L_WO + 24 * g4 * 192 + 12 * c16;
    u32x16 su = {wr + 192 * (g4 & 1) + 8 * p, wr + 384 + ((g4 & 1) && p >= 2 ? 0 : 32 * (g4 & 1) + 8 * p), (unsigned)(L_STAGE + 48 * head + (8 * g4 + q) * 576 + 192 * (p & 1) + 32 + 8 * (p >> 1)),
                 wol + wt[0], wol + wt[1], wol + wt[2], L_IDX + l16, wt[3] + l16, wt[4] + l16, wt[5] + l16};
    u32x2 t5 = {L_IDX + wt[10] + (((ln >> 3) & 1) * 2 + ((ln >> 2) & 1)) * 1024 + (g4 + 32 * (ln & 1)) * 16 + ((ln >> 1) & 1) * 8,
                (unsigned)(L_TBL + p * 1536 + (ln >> 2) * 4 + 96 * wid)};
    unsigned tbl = wt[9], wop = 1024 * wid;
    asm volatile("" : "+{s[48:63]}"(sp), "+{s[64:79]}"(tb0), "+{s[80:83]}"(tb1), "+s"(mo0), "+{s93}"(tbl));
    asm volatile("s_nop 3" : "+{v[120:127]}"(ad0), "+{v[128:131]}"(ad1), "+{v[32:47]}"(su), "+v"(tx), "+v"(t4), "+v"(t5));
#pragma unroll
    for (int i = 0; i < 3; i++) DMA(rsW, l16, (3072 - w0) + i * 1024, wld + (3072 - w0) + i * 1024, "");
    if (lane < 48) DMA(rsW, l16, 6144, wld + 6144, "");
    DMA(rsX, tx.x, tb0[0], sp[2], LD_AUX); DMA(rsX, tx.y, tb0[0], sp[2] + 1024, LD_AUX); DMA(rsX, tx.z, tb0[0], sp[2] + 2048, LD_AUX);
    DMA(rsM, t4.w, mo0, sp[6], LD_AUX); DMA(rsM, t4.w, mo0 + 1024, sp[6] + 1024, LD_AUX);
    asm volatile("s_waitcnt vmcnt(12)" : "+v"(id0), "+v"(id1), "+v"(rb0), "+v"(rb1), "+v"(warm) :: "memory");
    const float log2e = 1.4426950408889634f;
    {
        *(LDS float*)(smem + t5.y) = __uint_as_float(rb0 << 16) * log2e;
        *(LDS float*)(smem + t5.y + 768) = __uint_as_float(rb1 << 16) * log2e;
        auto tpos = [](unsigned i) { return __umul24(__umul24(i, 4370) >> 16, 9) + i; };
        *(LDS u32x2*)(smem + t5.x) = u32x2{tpos(id0.x) << 2 | tpos(id0.y) << 18, tpos(id0.z) << 2 | tpos(id0.w) << 18};
        *(LDS u32x2*)(smem + t5.x + 64) = u32x2{tpos(id1.x) << 2 | tpos(id1.y) << 18, tpos(id1.z) << 2 | tpos(id1.w) << 18};
    }
    asm volatile("s_waitcnt vmcnt(5)" ::: "memory");
    {
        const float cs = (float)(1.4426950408889634 / 4.898979485566356);
        LDS u32x4* a[3]; u32x4 v[3];
#pragma unroll
        for (int i = 0; i < 3; i++) { a[i] = (LDS u32x4*)(smem + t4[i]); v[i] = *a[i]; }
#pragma unroll
        for (int i = 0; i < 3; i++) *a[i] = scale8(v[i], cs);
    }
    WAIT_BAR(2);

    unsigned bad, hd2, gp2;
    u32x16 wr0, wr1, wr2; u32x8 wr3; u32x4 wr4; f32x16 bias[4];
    {
        asm volatile(
            "s_nop 4\n"
            "v_mov_b32 v64, 0\n"
            "s_mov_b32 exec_lo, 0x11000000\n"
            "s_mov_b32 exec_hi, 0x11000000\n"
            "v_mov_b32 v64, 0x3f803f80\n"
            "s_mov_b64 exec, -1\n"
            "v_mov_b32 v65, v64\n"
            "v_mov_b32 v66, v64\n"
            "v_mov_b32 v67, v64\n"
            "v_mov_b32 v68, v64\n"
            "v_mov_b32 v69, v64\n"
            "v_mov_b32 v70, v64\n"
            "v_mov_b32 v71, v64\n"
            "v_mov_b32 v72, v64\n"
            "v_mov_b32 v73, v64\n"
            "v_mov_b32 v74, v64\n"
            "v_mov_b32 v75, v64\n"
            "v_mov_b32 v76, v64\n"
            "v_mov_b32 v77, v64\n"
            "v_mov_b32 v78, v64\n"
            "v_mov_b32 v79, v64\n"
            "s_mov_b32 s97, 0\n"
            "s_mov_b32 s86, 0x53800000\n"
            "s_setprio 3\n"
            "s_mov_b32 m0, s94\n"
            "ds_read_b64_tr_b16 v[132:133], v32\n"
            "buffer_load_dwordx4 v128, s[88:91], s94 offen lds\n"
            "s_add_u32 s94, s94, 8192\n"
            "s_mov_b32 m0, s94\n"
            "ds_read_b64_tr_b16 v[134:135], v32 offset:2304\n"
            "ds_read_b128 v[104:107], v120\n"
            "ds_read_b64_tr_b16 v[156:157], v33\n"
            "ds_read_b64_tr_b16 v[158:159], v33 offset:2304\n"
            "ds_read_b64_tr_b16 v[180:181], v34\n"
            "ds_read_b64_tr_b16 v[182:183], v34 offset:2304\n"
            "ds_read_b128 v[112:115], v122\n"
            "v_add_u32 v24, s54, v128\n"
            "v_add_u32 v25, s56, v128\n"
            "s_waitcnt lgkmcnt(5)\n"
            "v_mfma_f32_32x32x16_bf16 v[0:15], v[132:135], v[104:107], 0\n"
            "ds_read_b64_tr_b16 v[136:137], v32 offset:9216\n"
            "ds_read_b64_tr_b16 v[138:139], v32 offset:11520\n"
            "s_setprio 2\n"
            "s_waitcnt lgkmcnt(5)\n"
            "v_mfma_f32_32x32x16_bf16 v[80:95], v[104:107], v[156:159], 0\n"
            "buffer_load_dwordx4 v128, s[88:91], s94 offen lds\n"
            "s_add_u32 s94, s94, 8192\n"
            "s_min_u32 s94, s94, 17408\n"
            "s_mov_b32 m0, s94\n"
            "s_waitcnt lgkmcnt(2)\n"
            "v_mfma_f32_16x16x32_bf16 v[96:99], v[180:183], v[112:115], 0\n"
            "ds_read_b128 v[108:111], v121\n"
            "ds_read_b64_tr_b16 v[160:161], v33 offset:9216\n"
            "ds_read_b64_tr_b16 v[162:163], v33 offset:11520\n"
            "ds_read_b128 v[116:119], v122 offset:3072\n"
            "ds_read_b64_tr_b16 v[140:141], v32 offset:18432\n"
            "ds_read_b64_tr_b16 v[142:143], v32 offset:20736\n"
            "s_waitcnt lgkmcnt(5)\n"
            "v_mfma_f32_32x32x16_bf16 v[0:15], v[136:139], v[108:111], v[0:15]\n"
            "ds_read_b128 v[104:107], v120 offset:64\n"
            "s_setprio 1\n"
            "ds_read_b64_tr_b16 v[164:165], v33 offset:18432\n"
            "s_waitcnt lgkmcnt(5)\n"
            "v_mfma_f32_32x32x16_bf16 v[80:95], v[108:111], v[160:163], v[80:95]\n"
            "ds_read_b64_tr_b16 v[166:167], v33 offset:20736\n"
            "ds_read_b64_tr_b16 v[184:185], v34 offset:18432\n"
            "s_waitcnt lgkmcnt(6)\n"
            "v_mfma_f32_16x16x32_bf16 v[100:103], v[180:183], v[116:119], 0\n"
            "ds_read_b64_tr_b16 v[186:187], v34 offset:20736\n"
            "ds_read_b128 v[112:115], v122 offset:64\n"
            "s_waitcnt lgkmcnt(5)\n"
            "v_mfma_f32_32x32x16_bf16 v[0:15], v[140:143], v[104:107], v[0:15]\n"
            "ds_read_b64_tr_b16 v[144:145], v32 offset:27648\n"
            "ds_read_b64_tr_b16 v[146:147], v32 offset:29952\n"
            "s_waitcnt lgkmcnt(5)\n"
            "v_mfma_f32_32x32x16_bf16 v[80:95], v[104:107], v[164:167], v[80:95]\n"
            "ds_read_b128 v[108:111], v121 offset:64\n"
            "s_setprio 0\n"
            "ds_read_b64_tr_b16 v[168:169], v33 offset:27648\n"
            "s_waitcnt lgkmcnt(4)\n"
            "v_mfma_f32_16x16x32_bf16 v[96:99], v[184:187], v[112:115], v[96:99]\n"
            "ds_read_b64_tr_b16 v[170:171], v33 offset:29952\n"
            "buffer_load_dwordx4 v128, s[88:91], s94 offen lds\n"
            "s_add_u32 m0, s50, 12288\n"
            "s_waitcnt lgkmcnt(2)\n"
            "v_mfma_f32_32x32x16_bf16 v[0:15], v[144:147], v[108:111], v[0:15]\n"
            "ds_read_b128 v[116:119], v122 offset:3136\n"
            "ds_read_b64_tr_b16 v[148:149], v32 offset:36864\n"
            "s_waitcnt lgkmcnt(2)\n"
            "v_mfma_f32_32x32x16_bf16 v[80:95], v[108:111], v[168:171], v[80:95]\n"
            "ds_read_b64_tr_b16 v[150:151], v32 offset:39168\n"
            "ds_read_b128 v[104:107], v120 offset:128\n"
            "s_setprio 3\n"
            "s_waitcnt lgkmcnt(3)\n"
            "v_mfma_f32_16x16x32_bf16 v[100:103], v[184:187], v[116:119], v[100:103]\n"
            "ds_read_b64_tr_b16 v[172:173], v33 offset:36864\n"
            "ds_read_b64_tr_b16 v[174:175], v33 offset:39168\n"
            "s_waitcnt lgkmcnt(2)\n"
            "v_mfma_f32_32x32x16_bf16 v[0:15], v[148:151], v[104:107], v[0:15]\n"
            "ds_read_b64_tr_b16 v[188:189], v34 offset:36864\n"
            "ds_read_b64_tr_b16 v[190:191], v34 offset:39168\n"
            "s_waitcnt lgkmcnt(2)\n"
            "v_mfma_f32_32x32x16_bf16 v[80:95], v[104:107], v[172:175], v[80:95]\n"
            "ds_read_b128 v[112:115], v122 offset:128\n"
            "ds_read_b64_tr_b16 v[152:153], v32 offset:46080\n"
            "ds_read_b64_tr_b16 v[154:155], v32 offset:48384\n"
            "ds_read_b128 v[108:111], v121 offset:128\n"
            "ds_read_b64_tr_b16 v[176:177], v33 offset:46080\n"
            "ds_read_b64_tr_b16 v[178:179], v33 offset:48384\n"
            "s_setprio 2\n"
            "s_waitcnt lgkmcnt(5)\n"
            "v_mfma_f32_16x16x32_bf16 v[96:99], v[188:191], v[112:115], v[96:99]\n"
            "ds_read_b128 v[48:51], v38\n"
            "ds_read_b128 v[52:55], v38 offset:1024\n"
            "s_waitcnt lgkmcnt(4)\n"
            "v_mfma_f32_32x32x16_bf16 v[0:15], v[152:155], v[108:111], v[0:15]\n"
            "ds_read_b128 v[116:119], v122 offset:3200\n"
            "buffer_load_dwordx4 v124, s[36:39], s65 offen nt lds\n"
            "s_add_u32 m0, s50, 13312\n"
            "s_waitcnt lgkmcnt(3)\n"
            "v_mfma_f32_32x32x16_bf16 v[80:95], v[108:111], v[176:179], v[80:95]\n"
            "ds_read_b128 v[104:107], v120 offset:6144\n"
            "ds_read_b128 v[56:59], v38 offset:2048\n"
            "s_waitcnt lgkmcnt(4)\n"
            "v_add_u32_sdwa v192, s93, v48 dst_sel:DWORD dst_unused:UNUSED_PAD src0_sel:DWORD src1_sel:WORD_0\n"
            "v_add_u32_sdwa v193, s93, v48 dst_sel:DWORD dst_unused:UNUSED_PAD src0_sel:DWORD src1_sel:WORD_1\n"
            "s_setprio 1\n"
            "v_add_u32_sdwa v194, s93, v49 dst_sel:DWORD dst_unused:UNUSED_PAD src0_sel:DWORD src1_sel:WORD_0\n"
            "v_add_u32_sdwa v195, s93, v49 dst_sel:DWORD dst_unused:UNUSED_PAD src0_sel:DWORD src1_sel:WORD_1\n"
            "s_waitcnt lgkmcnt(2)\n"
            "v_mfma_f32_16x16x32_bf16 v[100:103], v[188:191], v[116:119], v[100:103]\n"
            "ds_read_b128 v[60:63], v38 offset:3072\n"
            "ds_read_b128 v[112:115], v122 offset:6144\n"
            "v_add_u32_sdwa v196, s93, v50 dst_sel:DWORD dst_unused:UNUSED_PAD src0_sel:DWORD src1_sel:WORD_0\n"
            "v_add_u32_sdwa v197, s93, v50 dst_sel:DWORD dst_unused:UNUSED_PAD src0_sel:DWORD src1_sel:WORD_1\n"
            "v_add_u32_sdwa v198, s93, v51 dst_sel:DWORD dst_unused:UNUSED_PAD src0_sel:DWORD src1_sel:WORD_0\n"
            "v_add_u32_sdwa v199, s93, v51 dst_sel:DWORD dst_unused:UNUSED_PAD src0_sel:DWORD src1_sel:WORD_1\n"
            "v_add_u32_sdwa v200, s93, v52 dst_sel:DWORD dst_unused:UNUSED_PAD src0_sel:DWORD src1_sel:WORD_0\n"
            "v_add_u32_sdwa v201, s93, v52 dst_sel:DWORD dst_unused:UNUSED_PAD src0_sel:DWORD src1_sel:WORD_1\n"
            "v_add_u32_sdwa v202, s93, v53 dst_sel:DWORD dst_unused:UNUSED_PAD src0_sel:DWORD src1_sel:WORD_0\n"
            "v_add_u32_sdwa v203, s93, v53 dst_sel:DWORD dst_unused:UNUSED_PAD src0_sel:DWORD src1_sel:WORD_1\n"
            "v_add_u32_sdwa v204, s93, v54 dst_sel:DWORD dst_unused:UNUSED_PAD src0_sel:DWORD src1_sel:WORD_0\n"
            "v_add_u32_sdwa v205, s93, v54 dst_sel:DWORD dst_unused:UNUSED_PAD src0_sel:DWORD src1_sel:WORD_1\n"
            "v_add_u32_sdwa v206, s93, v55 dst_sel:DWORD dst_unused:UNUSED_PAD src0_sel:DWORD src1_sel:WORD_0\n"
            "v_add_u32_sdwa v207, s93, v55 dst_sel:DWORD dst_unused:UNUSED_PAD src0_sel:DWORD src1_sel:WORD_1\n"
            "ds_read_b128 v[48:51], v38 offset:4096\n"
            "ds_read_b128 v[52:55], v38 offset:5120\n"
            "s_mov_b64 exec, s[60:61]\n"
            "v_cvt_pk_bf16_f32 v64, v80, v81\n"
            "v_cvt_pk_bf16_f32 v65, v82, v83\n"
            "v_cvt_pk_bf16_f32 v66, v84, v85\n"
            "v_cvt_pk_bf16_f32 v67, v86, v87\n"
            "s_mov_b64 exec, -1\n"
            "v_cvt_pk_bf16_f32 v80, v0, v1\n"
            "v_cvt_pk_bf16_f32 v81, v2, v3\n"
            "v_cvt_pk_bf16_f32 v82, v4, v5\n"
            "s_setprio 0\n"
            "v_cvt_pk_bf16_f32 v83, v6, v7\n"
            "v_cvt_pk_bf16_f32 v84, v8, v9\n"
            "v_cvt_pk_bf16_f32 v85, v10, v11\n"
            "v_cvt_pk_bf16_f32 v86, v12, v13\n"
            "v_cvt_pk_bf16_f32 v87, v14, v15\n"
            "s_waitcnt lgkmcnt(5)\n"
            "v_mfma_f32_32x32x16_bf16 v[0:15], v[132:135], v[104:107], 0\n"
            "ds_read_b128 v[108:111], v121 offset:6144\n"
            "ds_read_b128 v[116:119], v122 offset:9216\n"
            "s_waitcnt lgkmcnt(6)\n"
            "v_add_u32_sdwa v208, s93, v56 dst_sel:DWORD dst_unused:UNUSED_PAD src0_sel:DWORD src1_sel:WORD_0\n"
            "v_add_u32_sdwa v209, s93, v56 dst_sel:DWORD dst_unused:UNUSED_PAD src0_sel:DWORD src1_sel:WORD_1\n"
            "v_add_u32_sdwa v210, s93, v57 dst_sel:DWORD dst_unused:UNUSED_PAD src0_sel:DWORD src1_sel:WORD_0\n"
            "v_add_u32_sdwa v211, s93, v57 dst_sel:DWORD dst_unused:UNUSED_PAD src0_sel:DWORD src1_sel:WORD_1\n"
            "v_add_u32_sdwa v212, s93, v58 dst_sel:DWORD dst_unused:UNUSED_PAD src0_sel:DWORD src1_sel:WORD_0\n"
            "v_add_u32_sdwa v213, s93, v58 dst_sel:DWORD dst_unused:UNUSED_PAD src0_sel:DWORD src1_sel:WORD_1\n"
            "v_add_u32_sdwa v214, s93, v59 dst_sel:DWORD dst_unused:UNUSED_PAD src0_sel:DWORD src1_sel:WORD_0\n"
            "v_add_u32_sdwa v215, s93, v59 dst_sel:DWORD dst_unused:UNUSED_PAD src0_sel:DWORD src1_sel:WORD_1\n"
            "s_waitcnt lgkmcnt(5)\n"
            "v_add_u32_sdwa v216, s93, v60 dst_sel:DWORD dst_unused:UNUSED_PAD src0_sel:DWORD src1_sel:WORD_0\n"
            "v_add_u32_sdwa v217, s93, v60 dst_sel:DWORD dst_unused:UNUSED_PAD src0_sel:DWORD src1_sel:WORD_1\n"
            "v_add_u32_sdwa v218, s93, v61 dst_sel:DWORD dst_unused:UNUSED_PAD src0_sel:DWORD src1_sel:WORD_0\n"
            "v_add_u32_sdwa v219, s93, v61 dst_sel:DWORD dst_unused:UNUSED_PAD src0_sel:DWORD src1_sel:WORD_1\n"
            "v_add_u32_sdwa v220, s93, v62 dst_sel:DWORD dst_unused:UNUSED_PAD src0_sel:DWORD src1_sel:WORD_0\n"
            "v_add_u32_sdwa v221, s93, v62 dst_sel:DWORD dst_unused:UNUSED_PAD src0_sel:DWORD src1_sel:WORD_1\n"
            "v_add_u32_sdwa v222, s93, v63 dst_sel:DWORD dst_unused:UNUSED_PAD src0_sel:DWORD src1_sel:WORD_0\n"
            "v_add_u32_sdwa v223, s93, v63 dst_sel:DWORD dst_unused:UNUSED_PAD src0_sel:DWORD src1_sel:WORD_1\n"
            "s_waitcnt lgkmcnt(3)\n"
            "v_add_u32_sdwa v224, s93, v48 dst_sel:DWORD dst_unused:UNUSED_PAD src0_sel:DWORD src1_sel:WORD_0\n"
            "v_add_u32_sdwa v225, s93, v48 dst_sel:DWORD dst_unused:UNUSED_PAD src0_sel:DWORD src1_sel:WORD_1\n"
            "v_add_u32_sdwa v226, s93, v49 dst_sel:DWORD dst_unused:UNUSED_PAD src0_sel:DWORD src1_sel:WORD_0\n"
            "v_add_u32_sdwa v227, s93, v49 dst_sel:DWORD dst_unused:UNUSED_PAD src0_sel:DWORD src1_sel:WORD_1\n"
            "v_add_u32_sdwa v228, s93, v50 dst_sel:DWORD dst_unused:UNUSED_PAD src0_sel:DWORD src1_sel:WORD_0\n"
            "s_setprio 3\n"
            "v_add_u32_sdwa v229, s93, v50 dst_sel:DWORD dst_unused:UNUSED_PAD src0_sel:DWORD src1_sel:WORD_1\n"
            "v_add_u32_sdwa v230, s93, v51 dst_sel:DWORD dst_unused:UNUSED_PAD src0_sel:DWORD src1_sel:WORD_0\n"
            "v_add_u32_sdwa v231, s93, v51 dst_sel:DWORD dst_unused:UNUSED_PAD src0_sel:DWORD src1_sel:WORD_1\n"
            "s_waitcnt lgkmcnt(2)\n"
            "v_add_u32_sdwa v232, s93, v52 dst_sel:DWORD dst_unused:UNUSED_PAD src0_sel:DWORD src1_sel:WORD_0\n"
            "v_add_u32_sdwa v233, s93, v52 dst_sel:DWORD dst_unused:UNUSED_PAD src0_sel:DWORD src1_sel:WORD_1\n"
            "v_add_u32_sdwa v234, s93, v53 dst_sel:DWORD dst_unused:UNUSED_PAD src0_sel:DWORD src1_sel:WORD_0\n"
            "v_add_u32_sdwa v235, s93, v53 dst_sel:DWORD dst_unused:UNUSED_PAD src0_sel:DWORD src1_sel:WORD_1\n"
            "ds_read_b128 v[56:59], v38 offset:6144\n"
            "ds_read_b128 v[60:63], v38 offset:7168\n"
            "v_mov_b32 v48, v35\n"
            "v_mov_b32 v49, v36\n"
            "v_mov_b32 v50, v37\n"
            "v_mov_b32 v51, v39\n"
            "v_mov_b32 v52, v40\n"
            "v_mov_b32 v53, v41\n"
            "v_mfma_f32_32x32x16_bf16 v[32:47], v[104:107], v[156:159], 0\n"
            "ds_read_b128 v[104:107], v120 offset:6208\n"
            "buffer_load_dwordx4 v125, s[36:39], s65 offen nt lds\n"
            "s_mov_b64 exec, s[60:61]\n"
            "v_cvt_pk_bf16_f32 v68, v88, v89\n"
            "v_cvt_pk_bf16_f32 v69, v90, v91\n"
            "v_cvt_pk_bf16_f32 v70, v92, v93\n"
            "v_cvt_pk_bf16_f32 v71, v94, v95\n"
            "s_mov_b64 exec, -1\n"
            "v_cvt_pk_bf16_f32 v88, v96, v97\n"
            "v_cvt_pk_bf16_f32 v89, v98, v99\n"
            "v_mfma_f32_16x16x32_bf16 v[96:99], v[180:183], v[112:115], 0\n"
            "ds_read_b128 v[112:115], v122 offset:6208\n"
            "s_setprio 2\n"
            "ds_read_b32 v192, v192\n"
            "v_cvt_pk_bf16_f32 v90, v100, v101\n"
            "s_waitcnt lgkmcnt(6)\n"
            "v_mfma_f32_32x32x16_bf16 v[0:15], v[136:139], v[108:111], v[0:15]\n"
            "ds_read_b32 v193, v193\n"
            "ds_read_b32 v194, v194\n"
            "v_cvt_pk_bf16_f32 v91, v102, v103\n"
            "s_add_u32 m0, s50, 14336\n"
            "v_add_u32_sdwa v236, s93, v54 dst_sel:DWORD dst_unused:UNUSED_PAD src0_sel:DWORD src1_sel:WORD_0\n"
            "v_add_u32_sdwa v237, s93, v54 dst_sel:DWORD dst_unused:UNUSED_PAD src0_sel:DWORD src1_sel:WORD_1\n"
            "v_add_u32_sdwa v238, s93, v55 dst_sel:DWORD dst_unused:UNUSED_PAD src0_sel:DWORD src1_sel:WORD_0\n"
            "v_mfma_f32_32x32x16_bf16 v[32:47], v[108:111], v[160:163], v[32:47]\n"
            "ds_read_b128 v[108:111], v121 offset:6208\n"
            "ds_read_b32 v195, v195\n"
            "v_add_u32_sdwa v239, s93, v55 dst_sel:DWORD dst_unused:UNUSED_PAD src0_sel:DWORD src1_sel:WORD_1\n"
            "s_waitcnt lgkmcnt(8)\n"
            "v_add_u32_sdwa v240, s93, v56 dst_sel:DWORD dst_unused:UNUSED_PAD src0_sel:DWORD src1_sel:WORD_0\n"
            "v_add_u32_sdwa v241, s93, v56 dst_sel:DWORD dst_unused:UNUSED_PAD src0_sel:DWORD src1_sel:WORD_1\n"
            "v_add_u32_sdwa v242, s93, v57 dst_sel:DWORD dst_unused:UNUSED_PAD src0_sel:DWORD src1_sel:WORD_0\n"
            "v_add_u32_sdwa v243, s93, v57 dst_sel:DWORD dst_unused:UNUSED_PAD src0_sel:DWORD src1_sel:WORD_1\n"
            "v_mfma_f32_16x16x32_bf16 v[100:103], v[180:183], v[116:119], 0\n"
            "ds_read_b128 v[116:119], v122 offset:9280\n"
            "buffer_load_dwordx4 v126, s[36:39], s65 offen nt lds\n"
            "s_waitcnt lgkmcnt(7)\n"
            "v_mfma_f32_32x32x16_bf16 v[0:15], v[140:143], v[104:107], v[0:15]\n"
            "ds_read_b32 v196, v196\n"
            "ds_read_b32 v197, v197\n"
            "v_add_u32_sdwa v244, s93, v58 dst_sel:DWORD dst_unused:UNUSED_PAD src0_sel:DWORD src1_sel:WORD_0\n"
            "v_add_u32_sdwa v245, s93, v58 dst_sel:DWORD dst_unused:UNUSED_PAD src0_sel:DWORD src1_sel:WORD_1\n"
            "v_add_u32_sdwa v246, s93, v59 dst_sel:DWORD dst_unused:UNUSED_PAD src0_sel:DWORD src1_sel:WORD_0\n"
            "s_setprio 1\n"
            "v_add_u32_sdwa v247, s93, v59 dst_sel:DWORD dst_unused:UNUSED_PAD src0_sel:DWORD src1_sel:WORD_1\n"
            "v_add_u32_sdwa v248, s93, v60 dst_sel:DWORD dst_unused:UNUSED_PAD src0_sel:DWORD src1_sel:WORD_0\n"
            "v_mfma_f32_32x32x16_bf16 v[32:47], v[104:107], v[164:167], v[32:47]\n"
            "ds_read_b128 v[104:107], v120 offset:6272\n"
            "ds_read_b32 v198, v198\n"
            "v_add_u32_sdwa v249, s93, v60 dst_sel:DWORD dst_unused:UNUSED_PAD src0_sel:DWORD src1_sel:WORD_1\n"
            "v_add_u32_sdwa v250, s93, v61 dst_sel:DWORD dst_unused:UNUSED_PAD src0_sel:DWORD src1_sel:WORD_0\n"
            "v_add_u32_sdwa v251, s93, v61 dst_sel:DWORD dst_unused:UNUSED_PAD src0_sel:DWORD src1_sel:WORD_1\n"
            "v_add_u32_sdwa v252, s93, v62 dst_sel:DWORD dst_unused:UNUSED_PAD src0_sel:DWORD src1_sel:WORD_0\n"
            "v_add_u32_sdwa v253, s93, v62 dst_sel:DWORD dst_unused:UNUSED_PAD src0_sel:DWORD src1_sel:WORD_1\n"
            "s_waitcnt lgkmcnt(10)\n"
            "v_mfma_f32_16x16x32_bf16 v[96:99], v[184:187], v[112:115], v[96:99]\n"
            "ds_read_b128 v[112:115], v122 offset:6272\n"
            "ds_read_b32 v199, v199\n"
            "s_waitcnt vmcnt(6)\n"
            "s_waitcnt lgkmcnt(8)\n"
            "v_mfma_f32_32x32x16_bf16 v[0:15], v[144:147], v[108:111], v[0:15]\n"
            "ds_read_b128 v[16:19], v24\n"
            "ds_read_b128 v[20:23], v24 offset:1024\n"
            "v_add_u32_sdwa v254, s93, v63 dst_sel:DWORD dst_unused:UNUSED_PAD src0_sel:DWORD src1_sel:WORD_0\n"
            "v_add_u32_sdwa v255, s93, v63 dst_sel:DWORD dst_unused:UNUSED_PAD src0_sel:DWORD src1_sel:WORD_1\n"
            "s_mov_b32 m0, s48\n"
            "v_permlane16_swap_b32 v88, v90\n"
            "v_mfma_f32_32x32x16_bf16 v[32:47], v[108:111], v[168:171], v[32:47]\n"
            "ds_read_b128 v[108:111], v121 offset:6272\n"
            "ds_read_b32 v200, v200\n"
            "v_permlane16_swap_b32 v89, v91\n"
            "s_movrels_b32 s92, s67\n"
            "s_movrels_b32 s93, s77\n"
            "s_setprio 0\n"
            "s_waitcnt lgkmcnt(10)\n"
            "v_mfma_f32_16x16x32_bf16 v[100:103], v[184:187], v[116:119], v[100:103]\n"
            "ds_read_b128 v[116:119], v122 offset:9344\n"
            "ds_read_b32 v201, v201\n"
            "s_waitcnt lgkmcnt(5)\n"
            "v_or3_b32 v16, v16, v17, v18\n"
            "v_mfma_f32_32x32x16_bf16 v[0:15], v[148:151], v[104:107], v[0:15]\n"
            "ds_read_b32 v202, v202\n"
            "ds_read_b32 v203, v203\n"
            "s_waitcnt lgkmcnt(6)\n"
            "v_or3_b32 v20, v20, v21, v22\n"
            "v_or3_b32 v16, v16, v19, v23\n"
            "v_or_b32 v16, v16, v20\n"
            "s_not_b64 exec, s[60:61]\n"
            "v_mov_b32 v156, 0\n"
            "v_mov_b32 v157, 0\n"
            "v_mov_b32 v158, 0\n"
            "v_mov_b32 v159, 0\n"
            "v_mov_b32 v160, 0\n"
            "v_mov_b32 v161, 0\n"
            "s_mov_b64 exec, -1\n"
            "v_mfma_f32_32x32x16_bf16 v[32:47], v[104:107], v[172:175], v[32:47]\n"
            "ds_read_b32 v204, v204\n"
            "ds_read_b32 v205, v205\n"
            "s_waitcnt vmcnt(3)\n"
            "s_not_b64 exec, s[60:61]\n"
            "v_mov_b32 v162, 0\n"
            "v_mov_b32 v163, 0\n"
            "v_mov_b32 v164, 0\n"
            "v_mov_b32 v165, 0\n"
            "v_mov_b32 v166, 0\n"
            "v_mov_b32 v167, 0\n"
            "s_mov_b64 exec, -1\n"
            "s_setprio 3\n"
            "v_mfma_f32_16x16x32_bf16 v[96:99], v[188:191], v[112:115], v[96:99]\n"
            "ds_read_b32 v206, v206\n"
            "ds_read_b32 v207, v207\n"
            "s_waitcnt lgkmcnt(7)\n"
            "v_mfma_f32_16x16x32_bf16 v[100:103], v[188:191], v[116:119], v[100:103]\n"
            "ds_read_b32 v208, v208\n"
            "ds_read_b32 v209, v209\n"
            "v_mfma_f32_32x32x16_bf16 v[0:15], v[152:155], v[108:111], v[0:15]\n"
            "ds_read_b32 v210, v210\n"
            "ds_read_b32 v211, v211\n"
            "s_not_b64 exec, s[60:61]\n"
            "v_mov_b32 v168, 0\n"
            "v_mov_b32 v169, 0\n"
            "v_mov_b32 v170, 0\n"
            "v_mov_b32 v171, 0\n"
            "v_mov_b32 v172, 0\n"
            "v_mov_b32 v173, 0\n"
            "s_mov_b64 exec, -1\n"
            "v_mfma_f32_32x32x16_bf16 v[32:47], v[108:111], v[176:179], v[32:47]\n"
            "ds_read_b32 v212, v212\n"
            "ds_read_b32 v213, v213\n"
            "ds_read_b32 v214, v214\n"
            "ds_read_b32 v215, v215\n"
            "ds_read_b32 v216, v216\n"
            "ds_read_b32 v217, v217\n"
            "s_setprio 2\n"
            "ds_read_b32 v218, v218\n"
            "ds_read_b32 v219, v219\n"
            "ds_read_b32 v220, v220\n"
            "ds_read_b32 v221, v221\n"
            "ds_read_b32 v222, v222\n"
            "ds_read_b32 v223, v223\n"
            "ds_read_b32 v224, v224\n"
            "ds_read_b32 v225, v225\n"
            "ds_read_b32 v226, v226\n"
            "ds_read_b32 v227, v227\n"
            "ds_read_b32 v228, v228\n"
            "ds_read_b32 v229, v229\n"
            "ds_read_b32 v230, v230\n"
            "ds_read_b32 v231, v231\n"
            "ds_read_b32 v232, v232\n"
            "ds_read_b32 v233, v233\n"
            "ds_read_b32 v234, v234\n"
            "ds_read_b32 v235, v235\n"
            "ds_read_b32 v236, v236\n"
            "ds_read_b32 v237, v237\n"
            "ds_read_b32 v238, v238\n"
            "ds_read_b32 v239, v239\n"
            "ds_read_b32 v240, v240\n"
            "ds_read_b32 v241, v241\n"
            "ds_read_b32 v242, v242\n"
            "ds_read_b32 v243, v243\n"
            "ds_read_b32 v244, v244\n"
            "ds_read_b32 v245, v245\n"
            "s_setprio 1\n"
            "ds_read_b32 v246, v246\n"
            "ds_read_b32 v247, v247\n"
            "ds_read_b32 v248, v248\n"
            "ds_read_b32 v249, v249\n"
            "ds_read_b32 v250, v250\n"
            "ds_read_b32 v251, v251\n"
            "ds_read_b32 v252, v252\n"
            "ds_read_b32 v253, v253\n"
            "ds_read_b32 v254, v254\n"
            "ds_read_b32 v255, v255\n"
            "ds_write_b32 v25, v16\n"
            "s_not_b64 exec, s[60:61]\n"
            "v_mov_b32 v174, 0\n"
            "v_mov_b32 v175, 0\n"
            "v_mov_b32 v176, 0\n"
            "v_mov_b32 v177, 0\n"
            "v_mov_b32 v178, 0\n"
            "v_mov_b32 v179, 0\n"
            "s_mov_b64 exec, -1\n"
            "v_add_u32 v16, s57, v128\n"
            "s_waitcnt lgkmcnt(0)\n"
            "s_barrier\n"
            "s_setprio 0\n"
            "ds_read_b128 v[16:19], v16\n"
            "s_waitcnt lgkmcnt(0)\n"
            "v_or3_b32 v16, v16, v17, v18\n"
            "v_or_b32 v16, v16, v19\n"
            "v_cmp_ne_u32 vcc, 0, v16\n"
            "s_xor_b32 m0, s54, s53\n"
            "s_nop 0\n"
            "buffer_load_dwordx4 v127, s[40:43], s76 offen nt lds\n"
            "buffer_load_dwordx4 v127, s[40:43], s76 offen offset:1024 nt lds\n"
            "s_setprio 3\n"
            "v_mfma_f32_32x32x16_bf16 v[16:31], v[84:87], v[80:83], v[192:207]\n"
            "v_cvt_pk_bf16_f32 v100, v100, v101\n"
            "v_cvt_pk_bf16_f32 v101, v102, v103\n"
            "v_cvt_pk_bf16_f32 v102, v96, v97\n"
            "v_cvt_pk_bf16_f32 v103, v98, v99\n"
            "v_cvt_pk_bf16_f32 v96, v8, v9\n"
            "v_cvt_pk_bf16_f32 v97, v10, v11\n"
            "v_mfma_f32_32x32x8_bf16 v[16:31], v[90:91], v[88:89], v[16:31]\n"
            "s_mov_b64 exec, s[60:61]\n"
            "v_cvt_pk_bf16_f32 v72, v32, v33\n"
            "v_cvt_pk_bf16_f32 v73, v34, v35\n"
            "v_cvt_pk_bf16_f32 v74, v36, v37\n"
            "v_cvt_pk_bf16_f32 v75, v38, v39\n"
            "s_mov_b64 exec, -1\n"
            "s_mov_b64 exec, s[60:61]\n"
            "v_cvt_pk_bf16_f32 v76, v40, v41\n"
            "v_cvt_pk_bf16_f32 v77, v42, v43\n"
            "v_cvt_pk_bf16_f32 v78, v44, v45\n"
            "v_cvt_pk_bf16_f32 v79, v46, v47\n"
            "s_mov_b64 exec, -1\n"
            "v_permlane16_swap_b32 v102, v100\n"
            "v_permlane16_swap_b32 v103, v101\n"
            "v_cvt_pk_bf16_f32 v98, v12, v13\n"
            "v_cvt_pk_bf16_f32 v99, v14, v15\n"
            "v_mov_b32 v104, v48\n"
            "v_mov_b32 v105, v49\n"
            "v_mov_b32 v106, v50\n"
            "v_mov_b32 v107, v51\n"
            "s_setprio 2\n"
            "v_mov_b32 v108, v52\n"
            "v_mov_b32 v109, v53\n"
            "v_cvt_pk_bf16_f32 v92, v0, v1\n"
            "v_cvt_pk_bf16_f32 v93, v2, v3\n"
            "v_cvt_pk_bf16_f32 v94, v4, v5\n"
            "v_cvt_pk_bf16_f32 v95, v6, v7\n"
            "s_cbranch_vccnz LM1_%=\n"
            "LM1_%=_r:\n"
            "v_mfma_f32_32x32x16_bf16 v[0:15], v[96:99], v[80:83], v[208:223]\n"
            "v_exp_f32 v16, v16\n"
            "v_exp_f32 v17, v17\n"
            "v_exp_f32 v18, v18\n"
            "v_mfma_f32_32x32x8_bf16 v[0:15], v[100:101], v[88:89], v[0:15]\n"
            "v_exp_f32 v19, v19\n"
            "v_exp_f32 v20, v20\n"
            "v_exp_f32 v21, v21\n"
            "v_mfma_f32_32x32x16_bf16 v[32:47], v[84:87], v[92:95], v[224:239]\n"
            "v_exp_f32 v22, v22\n"
            "v_exp_f32 v23, v23\n"
            "s_nop 5\n"
            "v_exp_f32 v0, v0\n"
            "v_mfma_f32_32x32x16_bf16 v[48:63], v[96:99], v[92:95], v[240:255]\n"
            "s_setprio 1\n"
            "v_exp_f32 v1, v1\n"
            "v_exp_f32 v2, v2\n"
            "v_exp_f32 v3, v3\n"
            "v_mfma_f32_32x32x8_bf16 v[32:47], v[90:91], v[102:103], v[32:47]\n"
            "v_exp_f32 v4, v4\n"
            "v_exp_f32 v5, v5\n"
            "v_exp_f32 v6, v6\n"
            "v_mfma_f32_32x32x8_bf16 v[48:63], v[100:101], v[102:103], v[48:63]\n"
            "LM1_%=_j:\n"
            "ds_read_u16 v80, v104\n"
            "ds_read_u16_d16_hi v92, v104 offset:192\n"
            "s_mov_b32 m0, s75\n"
            "ds_read_u16 v81, v104 offset:384\n"
            "buffer_load_dwordx4 v124, s[36:39], s66 offen nt lds\n"
            "ds_read_u16_d16_hi v93, v104 offset:576\n"
            "ds_read_u16 v82, v104 offset:768\n"
            "ds_read_u16_d16_hi v94, v104 offset:960\n"
            "ds_read_u16 v83, v104 offset:1152\n"
            "ds_read_u16_d16_hi v95, v104 offset:1344\n"
            "ds_read_u16 v84, v105\n"
            "ds_read_u16_d16_hi v96, v105 offset:192\n"
            "ds_read_u16 v85, v105 offset:384\n"
            "ds_read_u16_d16_hi v97, v105 offset:576\n"
            "ds_read_u16 v86, v105 offset:768\n"
            "s_setprio 0\n"
            "ds_read_u16_d16_hi v98, v105 offset:960\n"
            "ds_read_u16 v87, v105 offset:1152\n"
            "ds_read_u16_d16_hi v99, v105 offset:1344\n"
            "ds_read_u16 v88, v106\n"
            "ds_read_u16_d16_hi v100, v106 offset:192\n"
            "ds_read_u16 v89, v106 offset:384\n"
            "ds_read_u16_d16_hi v101, v106 offset:576\n"
            "ds_read_u16 v90, v106 offset:768\n"
            "ds_read_u16_d16_hi v102, v106 offset:960\n"
            "ds_read_u16 v91, v106 offset:1152\n"
            "ds_read_u16_d16_hi v103, v106 offset:1344\n"
            "v_exp_f32 v7, v7\n"
            "v_cvt_pk_bf16_f32 v0, v0, v1\n"
            "v_exp_f32 v8, v8\n"
            "v_exp_f32 v9, v9\n"
            "v_exp_f32 v10, v10\n"
            "v_exp_f32 v11, v11\n"
            "v_exp_f32 v12, v12\n"
            "v_exp_f32 v13, v13\n"
            "v_exp_f32 v14, v14\n"
            "v_exp_f32 v15, v15\n"
            "v_exp_f32 v24, v24\n"
            "s_setprio 3\n"
            "v_exp_f32 v25, v25\n"
            "v_exp_f32 v26, v26\n"
            "v_exp_f32 v27, v27\n"
            "v_exp_f32 v28, v28\n"
            "v_exp_f32 v29, v29\n"
            "v_exp_f32 v30, v30\n"
            "v_exp_f32 v31, v31\n"
            "v_cvt_pk_bf16_f32 v1, v2, v3\n"
            "v_cvt_pk_bf16_f32 v2, v4, v5\n"
            "v_cvt_pk_bf16_f32 v3, v6, v7\n"
            "s_cmp_eq_u32 s50, s51\n"
            "s_cselect_b32 s96, 0xffffa000, s59\n"
            "v_cvt_pk_bf16_f32 v4, v8, v9\n"
            "v_cvt_pk_bf16_f32 v5, v10, v11\n"
            "v_cvt_pk_bf16_f32 v6, v12, v13\n"
            "v_cvt_pk_bf16_f32 v7, v14, v15\n"
            "s_mov_b32 s55, s50\n"
            "v_cvt_pk_bf16_f32 v8, v16, v17\n"
            "v_cvt_pk_bf16_f32 v9, v18, v19\n"
            "v_cvt_pk_bf16_f32 v10, v20, v21\n"
            "v_cvt_pk_bf16_f32 v11, v22, v23\n"
            "v_cvt_pk_bf16_f32 v12, v24, v25\n"
            "v_cvt_pk_bf16_f32 v13, v26, v27\n"
            "v_cvt_pk_bf16_f32 v14, v28, v29\n"
            "s_setprio 2\n"
            "v_cvt_pk_bf16_f32 v15, v30, v31\n"
            "v_mfma_f32_32x32x16_bf16 v[16:31], v[72:75], v[0:3], 0\n"
            "s_add_u32 s50, s50, s96\n"
            "v_add_u32 v120, s96, v120\n"
            "v_add_u32 v121, s96, v121\n"
            "v_add_u32 v122, s96, v122\n"
            "s_mov_b32 s96, 0x07060100\n"
            "s_add_u32 m0, s75, 1024\n"
            "v_mfma_f32_32x32x16_bf16 v[16:31], v[76:79], v[4:7], v[16:31]\n"
            "buffer_load_dwordx4 v125, s[36:39], s66 offen nt lds\n"
            "s_waitcnt lgkmcnt(15)\n"
            "v_perm_b32 v80, v92, v80, s96\n"
            "v_perm_b32 v81, v93, v81, s96\n"
            "v_perm_b32 v82, v94, v82, s96\n"
            "v_perm_b32 v83, v95, v83, s96\n"
            "s_waitcnt lgkmcnt(14)\n"
            "v_perm_b32 v84, v96, v84, s96\n"
            "v_mfma_f32_32x32x16_bf16 v[16:31], v[64:67], v[8:11], v[16:31]\n"
            "s_waitcnt lgkmcnt(12)\n"
            "v_perm_b32 v85, v97, v85, s96\n"
            "s_waitcnt lgkmcnt(10)\n"
            "v_perm_b32 v86, v98, v86, s96\n"
            "s_waitcnt lgkmcnt(8)\n"
            "v_perm_b32 v87, v99, v87, s96\n"
            "s_waitcnt lgkmcnt(6)\n"
            "v_perm_b32 v88, v100, v88, s96\n"
            "s_waitcnt lgkmcnt(4)\n"
            "v_perm_b32 v89, v101, v89, s96\n"
            "s_waitcnt lgkmcnt(2)\n"
            "v_perm_b32 v90, v102, v90, s96\n"
            "v_mfma_f32_32x32x16_bf16 v[16:31], v[68:71], v[12:15], v[16:31]\n"
            "s_waitcnt lgkmcnt(0)\n"
            "v_perm_b32 v91, v103, v91, s96\n"
            "s_waitcnt vmcnt(4)\n"
            "s_barrier\n"
            "s_setprio 1\n"
            "ds_write_b128 v107, v[80:83]\n"
            "ds_read_b128 v[104:107], v120\n"
            "ds_read_b128 v[112:115], v122\n"
            "ds_write_b128 v108, v[84:87]\n"
            "ds_write_b128 v109, v[88:91]\n"
            "ds_read_b128 v[108:111], v121\n"
            "s_add_u32 m0, s75, 2048\n"
            "s_mov_b32 s87, 50331648\n"
            "s_xor_b32 s54, s54, s53\n"
            "v_exp_f32 v32, v32\n"
            "v_exp_f32 v33, v33\n"
            "v_exp_f32 v34, v34\n"
            "v_exp_f32 v35, v35\n"
            "s_waitcnt lgkmcnt(4)\n"
            "v_mfma_f32_32x32x16_bf16 v[0:15], v[132:135], v[104:107], 0\n"
            "buffer_load_dwordx4 v126, s[36:39], s66 offen nt lds\n"
            "s_xor_b32 m0, s54, s53\n"
            "v_exp_f32 v36, v36\n"
            "s_setprio 0\n"
            "v_exp_f32 v37, v37\n"
            "v_mfma_f32_32x32x16_bf16 v[80:95], v[104:107], v[156:159], 0\n"
            "ds_read_b128 v[116:119], v122 offset:3072\n"
            "ds_read_b128 v[104:107], v120 offset:64\n"
            "v_exp_f32 v38, v38\n"
            "v_exp_f32 v39, v39\n"
            "v_cvt_pk_bf16_f32 v32, v32, v33\n"
            "s_waitcnt lgkmcnt(5)\n"
            "v_mfma_f32_16x16x32_bf16 v[96:99], v[180:183], v[112:115], 0\n"
            "ds_read_b128 v[112:115], v122 offset:64\n"
            "v_exp_f32 v40, v40\n"
            "s_waitcnt lgkmcnt(3)\n"
            "v_mfma_f32_32x32x16_bf16 v[0:15], v[136:139], v[108:111], v[0:15]\n"
            "v_exp_f32 v41, v41\n"
            "v_exp_f32 v42, v42\n"
            "v_exp_f32 v43, v43\n"
            "v_mfma_f32_32x32x16_bf16 v[80:95], v[108:111], v[160:163], v[80:95]\n"
            "ds_read_b128 v[108:111], v121 offset:64\n"
            "v_exp_f32 v44, v44\n"
            "v_exp_f32 v45, v45\n"
            "v_exp_f32 v46, v46\n"
            "s_waitcnt lgkmcnt(3)\n"
            "v_mfma_f32_16x16x32_bf16 v[100:103], v[180:183], v[116:119], 0\n"
            "ds_read_b128 v[116:119], v122 offset:3136\n"
            "s_setprio 3\n"
            "v_exp_f32 v47, v47\n"
            "s_waitcnt lgkmcnt(3)\n"
            "v_mfma_f32_32x32x16_bf16 v[0:15], v[140:143], v[104:107], v[0:15]\n"
            "buffer_store_dword v129, v129, s[44:47], s87 offen\n"
            "v_cvt_pk_bf16_f32 v33, v34, v35\n"
            "v_exp_f32 v48, v48\n"
            "v_exp_f32 v49, v49\n"
            "v_mfma_f32_32x32x16_bf16 v[80:95], v[104:107], v[164:167], v[80:95]\n"
            "ds_read_b128 v[104:107], v120 offset:128\n"
            "v_exp_f32 v50, v50\n"
            "v_exp_f32 v51, v51\n"
            "v_exp_f32 v52, v52\n"
            "s_waitcnt lgkmcnt(3)\n"
            "v_mfma_f32_16x16x32_bf16 v[96:99], v[184:187], v[112:115], v[96:99]\n"
            "buffer_store_dword v129, v129, s[44:47], s87 offen\n"
            "v_cvt_pk_bf16_f32 v34, v36, v37\n"
            "s_waitcnt lgkmcnt(2)\n"
            "v_mfma_f32_32x32x16_bf16 v[0:15], v[144:147], v[108:111], v[0:15]\n"
            "ds_read_b128 v[112:115], v122 offset:128\n"
            "v_exp_f32 v53, v53\n"
            "v_exp_f32 v54, v54\n"
            "v_exp_f32 v55, v55\n"
            "v_mfma_f32_32x32x16_bf16 v[80:95], v[108:111], v[168:171], v[80:95]\n"
            "buffer_store_dword v129, v129, s[44:47], s87 offen\n"
            "s_setprio 2\n"
            "v_exp_f32 v56, v56\n"
            "v_exp_f32 v57, v57\n"
            "v_cvt_pk_bf16_f32 v35, v38, v39\n"
            "s_waitcnt lgkmcnt(2)\n"
            "v_mfma_f32_16x16x32_bf16 v[100:103], v[184:187], v[116:119], v[100:103]\n"
            "ds_read_b128 v[108:111], v121 offset:128\n"
            "ds_read_b128 v[116:119], v122 offset:3200\n"
            "v_cvt_pk_bf16_f32 v36, v40, v41\n"
            "s_waitcnt lgkmcnt(3)\n"
            "v_mfma_f32_32x32x16_bf16 v[0:15], v[148:151], v[104:107], v[0:15]\n"
            "buffer_store_dword v129, v129, s[44:47], s87 offen\n"
            "v_exp_f32 v58, v58\n"
            "v_exp_f32 v59, v59\n"
            "v_cvt_pk_bf16_f32 v37, v42, v43\n"
            "v_mfma_f32_32x32x16_bf16 v[80:95], v[104:107], v[172:175], v[80:95]\n"
            "ds_read_b128 v[104:107], v120 offset:6144\n"
            "v_exp_f32 v60, v60\n"
            "v_exp_f32 v61, v61\n"
            "v_exp_f32 v62, v62\n"
            "s_waitcnt lgkmcnt(3)\n"
            "v_mfma_f32_16x16x32_bf16 v[96:99], v[188:191], v[112:115], v[96:99]\n"
            "ds_read_b128 v[112:115], v122 offset:6144\n"
            "v_exp_f32 v63, v63\n"
            "s_waitcnt lgkmcnt(3)\n"
            "v_mfma_f32_32x32x16_bf16 v[0:15], v[152:155], v[108:111], v[0:15]\n"
            "s_setprio 1\n"
            "v_cvt_pk_bf16_f32 v38, v44, v45\n"
            "v_cvt_pk_bf16_f32 v39, v46, v47\n"
            "v_cvt_pk_bf16_f32 v40, v48, v49\n"
            "v_cvt_pk_bf16_f32 v41, v50, v51\n"
            "v_cvt_pk_bf16_f32 v42, v52, v53\n"
            "v_cvt_pk_bf16_f32 v43, v54, v55\n"
            "v_mfma_f32_32x32x16_bf16 v[80:95], v[108:111], v[176:179], v[80:95]\n"
            "ds_read_b128 v[108:111], v121 offset:6144\n"
            "v_cvt_pk_bf16_f32 v44, v56, v57\n"
            "v_cvt_pk_bf16_f32 v45, v58, v59\n"
            "v_cvt_pk_bf16_f32 v46, v60, v61\n"
            "v_cvt_pk_bf16_f32 v47, v62, v63\n"
            "v_rcp_f32 v29, v28\n"
            "s_waitcnt lgkmcnt(3)\n"
            "v_mfma_f32_16x16x32_bf16 v[100:103], v[188:191], v[116:119], v[100:103]\n"
            "ds_read_b128 v[116:119], v122 offset:9216\n"
            "v_mul_f32 v16, v16, v29\n"
            "v_mul_f32 v17, v17, v29\n"
            "v_mfma_f32_32x32x16_bf16 v[48:63], v[64:67], v[32:35], 0\n"
            "buffer_load_dwordx4 v127, s[40:43], s93 offen nt lds\n"
            "v_cvt_pk_bf16_f32 v16, v16, v17\n"
            "v_mul_f32 v18, v18, v29\n"
            "v_mul_f32 v19, v19, v29\n"
            "v_cvt_pk_bf16_f32 v17, v18, v19\n"
            "v_mul_f32 v20, v20, v29\n"
            "v_mfma_f32_32x32x16_bf16 v[48:63], v[68:71], v[36:39], v[48:63]\n"
            "ds_write_b64 v123, v[16:17]\n"
            "s_mov_b64 exec, s[60:61]\n"
            "v_cvt_pk_bf16_f32 v64, v80, v81\n"
            "v_cvt_pk_bf16_f32 v65, v82, v83\n"
            "v_cvt_pk_bf16_f32 v66, v84, v85\n"
            "v_cvt_pk_bf16_f32 v67, v86, v87\n"
            "s_mov_b64 exec, -1\n"
            "s_setprio 0\n"
            "v_mfma_f32_32x32x16_bf16 v[48:63], v[72:75], v[40:43], v[48:63]\n"
            "v_cvt_pk_bf16_f32 v80, v0, v1\n"
            "v_cvt_pk_bf16_f32 v81, v2, v3\n"
            "v_cvt_pk_bf16_f32 v82, v4, v5\n"
            "v_cvt_pk_bf16_f32 v83, v6, v7\n"
            "v_cvt_pk_bf16_f32 v84, v8, v9\n"
            "v_cvt_pk_bf16_f32 v85, v10, v11\n"
            "v_mfma_f32_32x32x16_bf16 v[48:63], v[76:79], v[44:47], v[48:63]\n"
            "v_cvt_pk_bf16_f32 v86, v12, v13\n"
            "v_cvt_pk_bf16_f32 v87, v14, v15\n"
            "v_mul_f32 v21, v21, v29\n"
            "v_cvt_pk_bf16_f32 v18, v20, v21\n"
            "v_mul_f32 v22, v22, v29\n"
            "v_mul_f32 v23, v23, v29\n"
            "s_waitcnt lgkmcnt(4)\n"
            "v_mfma_f32_32x32x16_bf16 v[0:15], v[132:135], v[104:107], 0\n"
            "s_mov_b64 exec, s[60:61]\n"
            "v_cvt_pk_bf16_f32 v68, v88, v89\n"
            "v_cvt_pk_bf16_f32 v69, v90, v91\n"
            "v_cvt_pk_bf16_f32 v70, v92, v93\n"
            "v_cvt_pk_bf16_f32 v71, v94, v95\n"
            "s_mov_b64 exec, -1\n"
            "v_mfma_f32_32x32x16_bf16 v[32:47], v[104:107], v[156:159], 0\n"
            "buffer_load_dwordx4 v127, s[40:43], s93 offen offset:1024 nt lds\n"
            "v_cvt_pk_bf16_f32 v88, v96, v97\n"
            "v_cvt_pk_bf16_f32 v89, v98, v99\n"
            "s_setprio 3\n"
            "s_mov_b32 m0, s55\n"
            "v_cvt_pk_bf16_f32 v90, v100, v101\n"
            "v_cvt_pk_bf16_f32 v91, v102, v103\n"
            "s_waitcnt lgkmcnt(3)\n"
            "v_mfma_f32_16x16x32_bf16 v[96:99], v[180:183], v[112:115], 0\n"
            "ds_read_b128 v[104:107], v120 offset:6208\n"
            "ds_read_b128 v[112:115], v122 offset:6208\n"
            "v_mul_f32 v24, v24, v29\n"
            "s_waitcnt lgkmcnt(4)\n"
            "v_mfma_f32_32x32x16_bf16 v[0:15], v[136:139], v[108:111], v[0:15]\n"
            "v_mul_f32 v25, v25, v29\n"
            "v_cvt_pk_bf16_f32 v19, v22, v23\n"
            "v_cvt_pk_bf16_f32 v20, v24, v25\n"
            "v_mul_f32 v26, v26, v29\n"
            "v_mul_f32 v27, v27, v29\n"
            "v_cvt_pk_bf16_f32 v21, v26, v27\n"
            "v_mfma_f32_32x32x16_bf16 v[32:47], v[108:111], v[160:163], v[32:47]\n"
            "ds_read_b128 v[108:111], v121 offset:6208\n"
            "ds_write_b64 v123, v[18:19] offset:64\n"
            "v_add_u32 v24, s54, v128\n"
            "v_rcp_f32 v61, v60\n"
            "v_add_u32 v25, s56, v128\n"
            "v_mul_f32 v48, v48, v61\n"
            "s_waitcnt lgkmcnt(5)\n"
            "v_mfma_f32_16x16x32_bf16 v[100:103], v[180:183], v[116:119], 0\n"
            "ds_write_b64 v123, v[20:21] offset:128\n"
            "v_mul_f32 v49, v49, v61\n"
            "v_cvt_pk_bf16_f32 v48, v48, v49\n"
            "s_waitcnt lgkmcnt(4)\n"
            "v_mfma_f32_32x32x16_bf16 v[0:15], v[140:143], v[104:107], v[0:15]\n"
            "ds_read_b128 v[116:119], v122 offset:9280\n"
            "v_mul_f32 v50, v50, v61\n"
            "v_mul_f32 v51, v51, v61\n"
            "s_setprio 2\n"
            "v_cvt_pk_bf16_f32 v49, v50, v51\n"
            "v_mul_f32 v52, v52, v61\n"
            "v_mul_f32 v53, v53, v61\n"
            "v_cvt_pk_bf16_f32 v50, v52, v53\n"
            "v_mfma_f32_32x32x16_bf16 v[32:47], v[104:107], v[164:167], v[32:47]\n"
            "buffer_load_dwordx4 v124, s[36:39], s92 offen nt lds\n"
            "s_waitcnt vmcnt(10)\n"
            "v_mul_f32 v54, v54, v61\n"
            "v_mul_f32 v55, v55, v61\n"
            "v_mul_f32 v56, v56, v61\n"
            "v_mul_f32 v57, v57, v61\n"
            "s_waitcnt lgkmcnt(4)\n"
            "v_mfma_f32_16x16x32_bf16 v[96:99], v[184:187], v[112:115], v[96:99]\n"
            "ds_read_b128 v[16:19], v24\n"
            "ds_read_b128 v[20:23], v24 offset:1024\n"
            "v_mul_f32 v58, v58, v61\n"
            "s_waitcnt lgkmcnt(5)\n"
            "v_mfma_f32_32x32x16_bf16 v[0:15], v[144:147], v[108:111], v[0:15]\n"
            "ds_read_b128 v[104:107], v120 offset:6272\n"
            "ds_write_b64 v123, v[48:49] offset:6144\n"
            "v_mul_f32 v59, v59, v61\n"
            "s_add_u32 m0, s55, 1024\n"
            "v_cvt_pk_bf16_f32 v51, v54, v55\n"
            "v_cvt_pk_bf16_f32 v52, v56, v57\n"
            "v_cvt_pk_bf16_f32 v53, v58, v59\n"
            "v_mfma_f32_32x32x16_bf16 v[32:47], v[108:111], v[168:171], v[32:47]\n"
            "ds_write_b64 v123, v[50:51] offset:6208\n"
            "s_add_u32 s48, s48, 1\n"
            "v_max_f32 v30, v28, v29\n"
            "v_cmp_nge_f32_e64 s[84:85], s86, v30\n"
            "s_waitcnt lgkmcnt(4)\n"
            "v_or3_b32 v16, v16, v17, v18\n"
            "s_waitcnt lgkmcnt(3)\n"
            "v_or3_b32 v20, v20, v21, v22\n"
            "v_or3_b32 v16, v16, v19, v23\n"
            "s_setprio 1\n"
            "v_mfma_f32_16x16x32_bf16 v[100:103], v[184:187], v[116:119], v[100:103]\n"
            "ds_write_b64 v123, v[52:53] offset:6272\n"
            "v_or_b32 v16, v16, v20\n"
            "v_max_f32 v62, v60, v61\n"
            "s_waitcnt lgkmcnt(3)\n"
            "v_mfma_f32_32x32x16_bf16 v[0:15], v[148:151], v[104:107], v[0:15]\n"
            "ds_write_b32 v25, v16\n"
            "v_add_u32 v16, s57, v128\n"
            "v_cmp_nge_f32 vcc, s86, v62\n"
            "s_or_b64 vcc, vcc, s[84:85]\n"
            "s_addc_u32 s97, s97, s97\n"
            "v_permlane16_swap_b32 v88, v90\n"
            "v_mfma_f32_32x32x16_bf16 v[32:47], v[104:107], v[172:175], v[32:47]\n"
            "buffer_load_dwordx4 v125, s[36:39], s92 offen nt lds\n"
            "ds_read_b128 v[112:115], v122 offset:6272\n"
            "ds_read_b128 v[116:119], v122 offset:9344\n"
            "ds_read_b128 v[108:111], v121 offset:6272\n"
            "s_add_u32 m0, s55, 2048\n"
            "v_permlane16_swap_b32 v89, v91\n"
            "s_waitcnt lgkmcnt(3)\n"
            "s_barrier\n"
            "s_setprio 0\n"
            "s_waitcnt lgkmcnt(2)\n"
            "v_mfma_f32_16x16x32_bf16 v[96:99], v[188:191], v[112:115], v[96:99]\n"
            "ds_read_b128 v[16:19], v16\n"
            "s_waitcnt lgkmcnt(2)\n"
            "v_mfma_f32_16x16x32_bf16 v[100:103], v[188:191], v[116:119], v[100:103]\n"
            "s_waitcnt lgkmcnt(1)\n"
            "v_mfma_f32_32x32x16_bf16 v[0:15], v[152:155], v[108:111], v[0:15]\n"
            "v_mfma_f32_32x32x16_bf16 v[32:47], v[108:111], v[176:179], v[32:47]\n"
            "s_waitcnt lgkmcnt(0)\n"
            "v_or3_b32 v16, v16, v17, v18\n"
            "buffer_load_dwordx4 v126, s[36:39], s92 offen nt lds\n"
            "s_mov_b32 m0, s48\n"
            "v_or_b32 v16, v16, v19\n"
            "s_movrels_b32 s87, s63\n"
            "s_add_u32 s62, s87, s52\n"
            "s_add_u32 s87, s87, s49\n"
            "s_movrels_b32 s92, s67\n"
            "s_movrels_b32 s93, s77\n"
            "s_add_u32 s95, s87, 98304\n"
            "s_add_u32 s94, s62, 98304\n"
            "v_cmp_ne_u32 vcc, 0, v16\n"
            ".p2align 5\n"
            "s_nop 0\n"
            "s_nop 0\n"
            "LP3_%=:\n"
            "s_setprio 3\n"
            "v_mfma_f32_32x32x16_bf16 v[16:31], v[84:87], v[80:83], v[192:207]\n"
            "ds_read_b128 v[104:107], v130\n"
            "ds_read_b128 v[108:111], v128\n"
            "v_cvt_pk_bf16_f32 v100, v100, v101\n"
            "v_cvt_pk_bf16_f32 v101, v102, v103\n"
            "v_cvt_pk_bf16_f32 v102, v96, v97\n"
            "v_cvt_pk_bf16_f32 v103, v98, v99\n"
            "v_cvt_pk_bf16_f32 v96, v8, v9\n"
            "v_mfma_f32_32x32x8_bf16 v[16:31], v[90:91], v[88:89], v[16:31]\n"
            "ds_read_b128 v[112:115], v128 offset:1024\n"
            "ds_read_b128 v[116:119], v128 offset:2048\n"
            "s_mov_b64 exec, s[60:61]\n"
            "v_cvt_pk_bf16_f32 v72, v32, v33\n"
            "v_cvt_pk_bf16_f32 v73, v34, v35\n"
            "v_cvt_pk_bf16_f32 v74, v36, v37\n"
            "v_cvt_pk_bf16_f32 v75, v38, v39\n"
            "s_mov_b64 exec, -1\n"
            "s_mov_b64 exec, s[60:61]\n"
            "v_cvt_pk_bf16_f32 v76, v40, v41\n"
            "v_cvt_pk_bf16_f32 v77, v42, v43\n"
            "v_cvt_pk_bf16_f32 v78, v44, v45\n"
            "v_cvt_pk_bf16_f32 v79, v46, v47\n"
            "s_mov_b64 exec, -1\n"
            "v_permlane16_swap_b32 v102, v100\n"
            "v_permlane16_swap_b32 v103, v101\n"
            "v_cvt_pk_bf16_f32 v97, v10, v11\n"
            "v_cvt_pk_bf16_f32 v98, v12, v13\n"
            "v_cvt_pk_bf16_f32 v99, v14, v15\n"
            "v_cvt_pk_bf16_f32 v92, v0, v1\n"
            "v_cvt_pk_bf16_f32 v93, v2, v3\n"
            "v_cvt_pk_bf16_f32 v94, v4, v5\n"
            "s_setprio 2\n"
            "v_cvt_pk_bf16_f32 v95, v6, v7\n"
            "s_cbranch_vccnz LM2_%=\n"
            "LM2_%=_r:\n"
            "v_mfma_f32_32x32x16_bf16 v[0:15], v[96:99], v[80:83], v[208:223]\n"
            "ds_read_b128 v[124:127], v130 offset:64\n"
            "v_exp_f32 v16, v16\n"
            "v_exp_f32 v17, v17\n"
            "v_exp_f32 v18, v18\n"
            "v_mfma_f32_32x32x8_bf16 v[0:15], v[100:101], v[88:89], v[0:15]\n"
            "v_exp_f32 v19, v19\n"
            "v_exp_f32 v20, v20\n"
            "v_exp_f32 v21, v21\n"
            "v_mfma_f32_32x32x16_bf16 v[32:47], v[84:87], v[92:95], v[224:239]\n"
            "v_exp_f32 v22, v22\n"
            "v_exp_f32 v23, v23\n"
            "s_nop 5\n"
            "v_exp_f32 v0, v0\n"
            "v_mfma_f32_32x32x16_bf16 v[48:63], v[96:99], v[92:95], v[240:255]\n"
            "v_exp_f32 v1, v1\n"
            "v_exp_f32 v2, v2\n"
            "s_setprio 1\n"
            "v_exp_f32 v3, v3\n"
            "v_mfma_f32_32x32x8_bf16 v[32:47], v[90:91], v[102:103], v[32:47]\n"
            "v_exp_f32 v4, v4\n"
            "v_exp_f32 v5, v5\n"
            "v_exp_f32 v6, v6\n"
            "v_mfma_f32_32x32x8_bf16 v[48:63], v[100:101], v[102:103], v[48:63]\n"
            "LM2_%=_j:\n"
            "s_waitcnt vmcnt(9)\n"
            "v_exp_f32 v7, v7\n"
            "v_cvt_pk_bf16_f32 v0, v0, v1\n"
            "v_exp_f32 v8, v8\n"
            "s_waitcnt lgkmcnt(3)\n"
            "v_mfma_f32_16x16x32_bf16 v[80:83], v[104:107], v[108:111], 0\n"
            "ds_read_b128 v[108:111], v128 offset:3072\n"
            "v_exp_f32 v9, v9\n"
            "s_waitcnt lgkmcnt(3)\n"
            "v_mfma_f32_16x16x32_bf16 v[84:87], v[104:107], v[112:115], 0\n"
            "ds_read_b128 v[112:115], v128 offset:4096\n"
            "v_exp_f32 v10, v10\n"
            "s_waitcnt lgkmcnt(3)\n"
            "v_mfma_f32_16x16x32_bf16 v[88:91], v[104:107], v[116:119], 0\n"
            "ds_read_b128 v[116:119], v128 offset:5120\n"
            "v_exp_f32 v11, v11\n"
            "v_exp_f32 v12, v12\n"
            "s_setprio 0\n"
            "v_exp_f32 v13, v13\n"
            "v_cvt_pk_bf16_f32 v1, v2, v3\n"
            "s_waitcnt lgkmcnt(2)\n"
            "v_mfma_f32_16x16x32_bf16 v[92:95], v[104:107], v[108:111], 0\n"
            "ds_read_b128 v[108:111], v128 offset:6144\n"
            "v_exp_f32 v14, v14\n"
            "s_waitcnt lgkmcnt(2)\n"
            "v_mfma_f32_16x16x32_bf16 v[96:99], v[104:107], v[112:115], 0\n"
            "ds_read_b128 v[112:115], v128 offset:7168\n"
            "v_exp_f32 v15, v15\n"
            "s_waitcnt lgkmcnt(2)\n"
            "v_mfma_f32_16x16x32_bf16 v[100:103], v[104:107], v[116:119], 0\n"
            "ds_read_b128 v[116:119], v128 offset:8192\n"
            "ds_read_b128 v[104:107], v130 offset:128\n"
            "v_exp_f32 v24, v24\n"
            "v_exp_f32 v25, v25\n"
            "v_exp_f32 v26, v26\n"
            "s_waitcnt lgkmcnt(3)\n"
            "v_mfma_f32_16x16x32_bf16 v[80:83], v[124:127], v[108:111], v[80:83]\n"
            "ds_read_b128 v[108:111], v128 offset:9216\n"
            "v_exp_f32 v27, v27\n"
            "s_waitcnt lgkmcnt(3)\n"
            "v_mfma_f32_16x16x32_bf16 v[84:87], v[124:127], v[112:115], v[84:87]\n"
            "ds_read_b128 v[112:115], v128 offset:10240\n"
            "v_exp_f32 v28, v28\n"
            "s_waitcnt lgkmcnt(3)\n"
            "v_mfma_f32_16x16x32_bf16 v[88:91], v[124:127], v[116:119], v[88:91]\n"
            "ds_read_b128 v[116:119], v128 offset:11264\n"
            "v_exp_f32 v29, v29\n"
            "v_exp_f32 v30, v30\n"
            "s_setprio 3\n"
            "v_exp_f32 v31, v31\n"
            "v_cvt_pk_bf16_f32 v2, v4, v5\n"
            "s_waitcnt lgkmcnt(2)\n"
            "v_mfma_f32_16x16x32_bf16 v[92:95], v[124:127], v[108:111], v[92:95]\n"
            "ds_read_b128 v[108:111], v128 offset:12288\n"
            "v_cvt_pk_bf16_f32 v3, v6, v7\n"
            "v_cvt_pk_bf16_f32 v4, v8, v9\n"
            "s_waitcnt lgkmcnt(2)\n"
            "v_mfma_f32_16x16x32_bf16 v[96:99], v[124:127], v[112:115], v[96:99]\n"
            "ds_read_b128 v[112:115], v128 offset:13312\n"
            "v_cvt_pk_bf16_f32 v5, v10, v11\n"
            "v_cvt_pk_bf16_f32 v6, v12, v13\n"
            "s_waitcnt lgkmcnt(2)\n"
            "v_mfma_f32_16x16x32_bf16 v[100:103], v[124:127], v[116:119], v[100:103]\n"
            "ds_read_b128 v[116:119], v128 offset:14336\n"
            "ds_read_b128 v[124:127], v128 offset:15360\n"
            "v_cvt_pk_bf16_f32 v7, v14, v15\n"
            "v_cvt_pk_bf16_f32 v8, v16, v17\n"
            "v_cvt_pk_bf16_f32 v9, v18, v19\n"
            "v_cvt_pk_bf16_f32 v10, v20, v21\n"
            "v_cvt_pk_bf16_f32 v11, v22, v23\n"
            "v_cvt_pk_bf16_f32 v12, v24, v25\n"
            "s_waitcnt lgkmcnt(3)\n"
            "v_mfma_f32_16x16x32_bf16 v[80:83], v[104:107], v[108:111], v[80:83]\n"
            "ds_read_b128 v[108:111], v128 offset:16384\n"
            "v_cvt_pk_bf16_f32 v13, v26, v27\n"
            "v_cvt_pk_bf16_f32 v14, v28, v29\n"
            "s_waitcnt lgkmcnt(3)\n"
            "v_mfma_f32_16x16x32_bf16 v[84:87], v[104:107], v[112:115], v[84:87]\n"
            "ds_read_b128 v[112:115], v128 offset:17408\n"
            "v_cvt_pk_bf16_f32 v15, v30, v31\n"
            "s_xor_b32 s54, s54, s53\n"
            "s_waitcnt lgkmcnt(3)\n"
            "v_mfma_f32_16x16x32_bf16 v[88:91], v[104:107], v[116:119], v[88:91]\n"
            "s_barrier\n"
            "s_setprio 2\n"
            "s_waitcnt lgkmcnt(2)\n"
            "v_mfma_f32_16x16x32_bf16 v[92:95], v[104:107], v[124:127], v[92:95]\n"
            "v_cvt_pk_bf16_f32 v124, v80, v84\n"
            "v_cvt_pk_bf16_f32 v116, v81, v85\n"
            "s_waitcnt lgkmcnt(1)\n"
            "v_mfma_f32_16x16x32_bf16 v[96:99], v[104:107], v[108:111], v[96:99]\n"
            "s_cmp_eq_u32 s50, s51\n"
            "s_cselect_b32 s96, 0xffffa000, s59\n"
            "s_waitcnt lgkmcnt(0)\n"
            "v_mfma_f32_16x16x32_bf16 v[100:103], v[104:107], v[112:115], v[100:103]\n"
            "v_cvt_pk_bf16_f32 v125, v88, v92\n"
            "v_cvt_pk_bf16_f32 v117, v89, v93\n"
            "v_mfma_f32_32x32x16_bf16 v[16:31], v[72:75], v[0:3], 0\n"
            "v_add_u32 v120, s96, v120\n"
            "v_add_u32 v122, s96, v122\n"
            "v_add_u32 v121, s96, v121\n"
            "v_exp_f32 v32, v32\n"
            "s_nop 0\n"
            "v_cvt_pk_bf16_f32 v126, v96, v100\n"
            "v_mfma_f32_32x32x16_bf16 v[16:31], v[76:79], v[4:7], v[16:31]\n"
            "buffer_store_dwordx3 v[124:126], v129, s[44:47], s87 offen sc1\n"
            "s_setprio 1\n"
            "v_cvt_pk_bf16_f32 v118, v97, v101\n"
            "v_cvt_pk_bf16_f32 v98, v98, v102\n"
            "v_cvt_pk_bf16_f32 v96, v82, v86\n"
            "v_cvt_pk_bf16_f32 v97, v90, v94\n"
            "v_cvt_pk_bf16_f32 v100, v83, v87\n"
            "v_mfma_f32_32x32x16_bf16 v[16:31], v[64:67], v[8:11], v[16:31]\n"
            "ds_read_b128 v[104:107], v120\n"
            "ds_read_b128 v[112:115], v122\n"
            "v_cvt_pk_bf16_f32 v101, v91, v95\n"
            "v_cvt_pk_bf16_f32 v102, v99, v103\n"
            "v_exp_f32 v33, v33\n"
            "s_xor_b32 m0, s54, s53\n"
            "v_mfma_f32_32x32x16_bf16 v[16:31], v[68:71], v[12:15], v[16:31]\n"
            "ds_read_b128 v[108:111], v121\n"
            "ds_read_b128 v[124:127], v128 offset:18432\n"
            "v_exp_f32 v34, v34\n"
            "v_exp_f32 v35, v35\n"
            "v_exp_f32 v36, v36\n"
            "s_waitcnt lgkmcnt(3)\n"
            "v_mfma_f32_32x32x16_bf16 v[0:15], v[132:135], v[104:107], 0\n"
            "buffer_store_dwordx3 v[116:118], v129, s[44:47], s62 offen sc1\n"
            "v_exp_f32 v37, v37\n"
            "v_exp_f32 v38, v38\n"
            "v_cvt_pk_bf16_f32 v32, v32, v33\n"
            "v_mfma_f32_32x32x16_bf16 v[80:95], v[104:107], v[156:159], 0\n"
            "ds_read_b128 v[116:119], v122 offset:3072\n"
            "s_setprio 0\n"
            "ds_read_b128 v[104:107], v120 offset:64\n"
            "v_exp_f32 v39, v39\n"
            "v_exp_f32 v40, v40\n"
            "v_exp_f32 v41, v41\n"
            "v_exp_f32 v42, v42\n"
            "v_exp_f32 v43, v43\n"
            "v_exp_f32 v44, v44\n"
            "v_exp_f32 v45, v45\n"
            "v_exp_f32 v46, v46\n"
            "buffer_store_dwordx3 v[96:98], v129, s[44:47], s95 offen sc1\n"
            "v_exp_f32 v47, v47\n"
            "s_waitcnt lgkmcnt(4)\n"
            "v_mfma_f32_16x16x32_bf16 v[96:99], v[180:183], v[112:115], 0\n"
            "ds_read_b128 v[112:115], v122 offset:64\n"
            "v_cvt_pk_bf16_f32 v33, v34, v35\n"
            "v_cvt_pk_bf16_f32 v34, v36, v37\n"
            "s_waitcnt lgkmcnt(4)\n"
            "v_mfma_f32_32x32x16_bf16 v[0:15], v[136:139], v[108:111], v[0:15]\n"
            "v_exp_f32 v48, v48\n"
            "v_exp_f32 v49, v49\n"
            "v_exp_f32 v50, v50\n"
            "s_setprio 3\n"
            "v_mfma_f32_32x32x16_bf16 v[80:95], v[108:111], v[160:163], v[80:95]\n"
            "ds_read_b128 v[108:111], v121 offset:64\n"
            "v_exp_f32 v51, v51\n"
            "v_exp_f32 v52, v52\n"
            "v_exp_f32 v53, v53\n"
            "v_exp_f32 v54, v54\n"
            "buffer_store_dwordx3 v[100:102], v129, s[44:47], s94 offen sc1\n"
            "v_exp_f32 v55, v55\n"
            "s_waitcnt lgkmcnt(3)\n"
            "v_mfma_f32_16x16x32_bf16 v[100:103], v[180:183], v[116:119], 0\n"
            "ds_read_b128 v[116:119], v122 offset:3136\n"
            "v_exp_f32 v56, v56\n"
            "s_waitcnt lgkmcnt(3)\n"
            "v_mfma_f32_32x32x16_bf16 v[0:15], v[140:143], v[104:107], v[0:15]\n"
            "v_exp_f32 v57, v57\n"
            "v_exp_f32 v58, v58\n"
            "v_exp_f32 v59, v59\n"
            "v_mfma_f32_32x32x16_bf16 v[80:95], v[104:107], v[164:167], v[80:95]\n"
            "ds_read_b128 v[104:107], v120 offset:128\n"
            "v_exp_f32 v60, v60\n"
            "v_exp_f32 v61, v61\n"
            "s_setprio 2\n"
            "v_exp_f32 v62, v62\n"
            "s_waitcnt lgkmcnt(3)\n"
            "v_mfma_f32_16x16x32_bf16 v[96:99], v[184:187], v[112:115], v[96:99]\n"
            "ds_read_b128 v[112:115], v122 offset:128\n"
            "v_exp_f32 v63, v63\n"
            "s_waitcnt lgkmcnt(3)\n"
            "v_mfma_f32_32x32x16_bf16 v[0:15], v[144:147], v[108:111], v[0:15]\n"
            "v_cvt_pk_bf16_f32 v35, v38, v39\n"
            "v_cvt_pk_bf16_f32 v36, v40, v41\n"
            "v_cvt_pk_bf16_f32 v37, v42, v43\n"
            "v_cvt_pk_bf16_f32 v38, v44, v45\n"
            "v_cvt_pk_bf16_f32 v39, v46, v47\n"
            "v_cvt_pk_bf16_f32 v40, v48, v49\n"
            "v_mfma_f32_32x32x16_bf16 v[80:95], v[108:111], v[168:171], v[80:95]\n"
            "ds_read_b128 v[108:111], v121 offset:128\n"
            "buffer_load_dwordx4 v127, s[40:43], s93 offen nt lds\n"
            "v_cvt_pk_bf16_f32 v41, v50, v51\n"
            "v_cvt_pk_bf16_f32 v42, v52, v53\n"
            "v_cvt_pk_bf16_f32 v43, v54, v55\n"
            "v_cvt_pk_bf16_f32 v44, v56, v57\n"
            "s_waitcnt lgkmcnt(3)\n"
            "v_mfma_f32_16x16x32_bf16 v[100:103], v[184:187], v[116:119], v[100:103]\n"
            "ds_read_b128 v[116:119], v122 offset:3200\n"
            "v_cvt_pk_bf16_f32 v45, v58, v59\n"
            "v_cvt_pk_bf16_f32 v46, v60, v61\n"
            "s_waitcnt lgkmcnt(3)\n"
            "v_mfma_f32_32x32x16_bf16 v[0:15], v[148:151], v[104:107], v[0:15]\n"
            "v_cvt_pk_bf16_f32 v47, v62, v63\n"
            "s_mov_b32 s55, s50\n"
            "v_rcp_f32 v29, v28\n"
            "s_setprio 1\n"
            "s_add_u32 s48, s48, 1\n"
            "v_mul_f32 v16, v16, v29\n"
            "v_mfma_f32_32x32x16_bf16 v[80:95], v[104:107], v[172:175], v[80:95]\n"
            "ds_read_b128 v[104:107], v120 offset:6144\n"
            "v_mul_f32 v17, v17, v29\n"
            "v_cvt_pk_bf16_f32 v16, v16, v17\n"
            "v_mul_f32 v18, v18, v29\n"
            "v_mul_f32 v19, v19, v29\n"
            "v_cvt_pk_bf16_f32 v17, v18, v19\n"
            "v_mul_f32 v20, v20, v29\n"
            "s_waitcnt lgkmcnt(3)\n"
            "v_mfma_f32_16x16x32_bf16 v[96:99], v[188:191], v[112:115], v[96:99]\n"
            "ds_read_b128 v[112:115], v122 offset:6144\n"
            "ds_write_b64 v123, v[16:17]\n"
            "v_mul_f32 v21, v21, v29\n"
            "s_waitcnt lgkmcnt(4)\n"
            "v_mfma_f32_32x32x16_bf16 v[0:15], v[152:155], v[108:111], v[0:15]\n"
            "v_cvt_pk_bf16_f32 v18, v20, v21\n"
            "v_mul_f32 v22, v22, v29\n"
            "v_mul_f32 v23, v23, v29\n"
            "v_mul_f32 v24, v24, v29\n"
            "v_mul_f32 v25, v25, v29\n"
            "v_cvt_pk_bf16_f32 v19, v22, v23\n"
            "v_mfma_f32_32x32x16_bf16 v[80:95], v[108:111], v[176:179], v[80:95]\n"
            "ds_read_b128 v[108:111], v121 offset:6144\n"
            "buffer_load_dwordx4 v127, s[40:43], s93 offen offset:1024 nt lds\n"
            "s_mov_b32 m0, s55\n"
            "v_cvt_pk_bf16_f32 v20, v24, v25\n"
            "v_mul_f32 v26, v26, v29\n"
            "v_mul_f32 v27, v27, v29\n"
            "s_waitcnt lgkmcnt(4)\n"
            "v_mfma_f32_16x16x32_bf16 v[100:103], v[188:191], v[116:119], v[100:103]\n"
            "ds_read_b128 v[116:119], v122 offset:9216\n"
            "s_setprio 0\n"
            "ds_write_b64 v123, v[18:19] offset:64\n"
            "v_cvt_pk_bf16_f32 v21, v26, v27\n"
            "v_mfma_f32_32x32x16_bf16 v[48:63], v[64:67], v[32:35], 0\n"
            "ds_write_b64 v123, v[20:21] offset:128\n"
            "s_mov_b64 exec, s[60:61]\n"
            "v_cvt_pk_bf16_f32 v64, v80, v81\n"
            "v_cvt_pk_bf16_f32 v65, v82, v83\n"
            "v_cvt_pk_bf16_f32 v66, v84, v85\n"
            "v_cvt_pk_bf16_f32 v67, v86, v87\n"
            "s_mov_b64 exec, -1\n"
            "v_mfma_f32_32x32x16_bf16 v[48:63], v[68:71], v[36:39], v[48:63]\n"
            "v_cvt_pk_bf16_f32 v80, v0, v1\n"
            "v_cvt_pk_bf16_f32 v81, v2, v3\n"
            "v_cvt_pk_bf16_f32 v82, v4, v5\n"
            "v_cvt_pk_bf16_f32 v83, v6, v7\n"
            "v_cvt_pk_bf16_f32 v84, v8, v9\n"
            "v_cvt_pk_bf16_f32 v85, v10, v11\n"
            "v_mfma_f32_32x32x16_bf16 v[48:63], v[72:75], v[40:43], v[48:63]\n"
            "v_cvt_pk_bf16_f32 v86, v12, v13\n"
            "v_cvt_pk_bf16_f32 v87, v14, v15\n"
            "v_add_u32 v24, s54, v128\n"
            "v_add_u32 v25, s56, v128\n"
            "v_max_f32 v30, v28, v29\n"
            "v_cmp_nge_f32_e64 s[84:85], s86, v30\n"
            "v_mfma_f32_32x32x16_bf16 v[48:63], v[76:79], v[44:47], v[48:63]\n"
            "buffer_load_dwordx4 v124, s[36:39], s92 offen nt lds\n"
            "s_waitcnt vmcnt(10)\n"
            "s_add_u32 m0, s55, 1024\n"
            "s_add_u32 s50, s50, s96\n"
            "s_setprio 3\n"
            "s_mov_b64 exec, s[60:61]\n"
            "v_cvt_pk_bf16_f32 v68, v88, v89\n"
            "v_cvt_pk_bf16_f32 v69, v90, v91\n"
            "v_cvt_pk_bf16_f32 v70, v92, v93\n"
            "v_cvt_pk_bf16_f32 v71, v94, v95\n"
            "s_mov_b64 exec, -1\n"
            "s_waitcnt lgkmcnt(6)\n"
            "v_mfma_f32_32x32x16_bf16 v[0:15], v[132:135], v[104:107], 0\n"
            "ds_read_b128 v[16:19], v24\n"
            "ds_read_b128 v[20:23], v24 offset:1024\n"
            "v_cvt_pk_bf16_f32 v88, v96, v97\n"
            "v_cvt_pk_bf16_f32 v89, v98, v99\n"
            "v_cvt_pk_bf16_f32 v90, v100, v101\n"
            "v_cvt_pk_bf16_f32 v91, v102, v103\n"
            "v_mfma_f32_32x32x16_bf16 v[32:47], v[104:107], v[156:159], 0\n"
            "ds_read_b128 v[104:107], v120 offset:6208\n"
            "v_rcp_f32 v61, v60\n"
            "v_permlane16_swap_b32 v88, v90\n"
            "v_mul_f32 v48, v48, v61\n"
            "v_mul_f32 v49, v49, v61\n"
            "s_waitcnt lgkmcnt(8)\n"
            "v_mfma_f32_16x16x32_bf16 v[96:99], v[180:183], v[112:115], 0\n"
            "ds_read_b128 v[112:115], v122 offset:6208\n"
            "v_cvt_pk_bf16_f32 v48, v48, v49\n"
            "v_mul_f32 v50, v50, v61\n"
            "s_waitcnt lgkmcnt(7)\n"
            "v_mfma_f32_32x32x16_bf16 v[0:15], v[136:139], v[108:111], v[0:15]\n"
            "v_mul_f32 v51, v51, v61\n"
            "v_cvt_pk_bf16_f32 v49, v50, v51\n"
            "v_mul_f32 v52, v52, v61\n"
            "v_mul_f32 v53, v53, v61\n"
            "s_waitcnt lgkmcnt(3)\n"
            "v_or3_b32 v16, v16, v17, v18\n"
            "s_setprio 2\n"
            "v_cvt_pk_bf16_f32 v50, v52, v53\n"
            "v_mfma_f32_32x32x16_bf16 v[32:47], v[108:111], v[160:163], v[32:47]\n"
            "ds_read_b128 v[108:111], v121 offset:6208\n"
            "ds_write_b64 v123, v[48:49] offset:6144\n"
            "v_mul_f32 v54, v54, v61\n"
            "v_mul_f32 v55, v55, v61\n"
            "v_mul_f32 v56, v56, v61\n"
            "v_mul_f32 v57, v57, v61\n"
            "v_mul_f32 v58, v58, v61\n"
            "v_mfma_f32_16x16x32_bf16 v[100:103], v[180:183], v[116:119], 0\n"
            "ds_read_b128 v[116:119], v122 offset:9280\n"
            "buffer_load_dwordx4 v125, s[36:39], s92 offen nt lds\n"
            "s_waitcnt lgkmcnt(4)\n"
            "v_mfma_f32_32x32x16_bf16 v[0:15], v[140:143], v[104:107], v[0:15]\n"
            "v_mul_f32 v59, v59, v61\n"
            "v_or3_b32 v20, v20, v21, v22\n"
            "v_or3_b32 v16, v16, v19, v23\n"
            "v_cvt_pk_bf16_f32 v51, v54, v55\n"
            "v_cvt_pk_bf16_f32 v52, v56, v57\n"
            "v_cvt_pk_bf16_f32 v53, v58, v59\n"
            "v_mfma_f32_32x32x16_bf16 v[32:47], v[104:107], v[164:167], v[32:47]\n"
            "ds_read_b128 v[104:107], v120 offset:6272\n"
            "ds_write_b64 v123, v[50:51] offset:6208\n"
            "v_or_b32 v16, v16, v20\n"
            "s_add_u32 m0, s55, 2048\n"
            "v_max_f32 v62, v60, v61\n"
            "v_permlane16_swap_b32 v89, v91\n"
            "s_waitcnt lgkmcnt(5)\n"
            "v_mfma_f32_16x16x32_bf16 v[96:99], v[184:187], v[112:115], v[96:99]\n"
            "ds_write_b64 v123, v[52:53] offset:6272\n"
            "v_cmp_nge_f32 vcc, s86, v62\n"
            "s_or_b64 vcc, vcc, s[84:85]\n"
            "s_addc_u32 s97, s97, s97\n"
            "s_setprio 1\n"
            "s_waitcnt lgkmcnt(5)\n"
            "v_mfma_f32_32x32x16_bf16 v[0:15], v[144:147], v[108:111], v[0:15]\n"
            "ds_write_b32 v25, v16\n"
            "v_add_u32 v16, s57, v128\n"
            "v_mfma_f32_32x32x16_bf16 v[32:47], v[108:111], v[168:171], v[32:47]\n"
            "ds_read_b128 v[112:115], v122 offset:6272\n"
            "ds_read_b128 v[108:111], v121 offset:6272\n"
            "s_waitcnt lgkmcnt(6)\n"
            "v_mfma_f32_16x16x32_bf16 v[100:103], v[184:187], v[116:119], v[100:103]\n"
            "ds_read_b128 v[116:119], v122 offset:9344\n"
            "buffer_load_dwordx4 v126, s[36:39], s92 offen nt lds\n"
            "s_waitcnt lgkmcnt(6)\n"
            "v_mfma_f32_32x32x16_bf16 v[0:15], v[148:151], v[104:107], v[0:15]\n"
            "s_mov_b32 m0, s48\n"
            "s_nop 0\n"
            "s_movrels_b32 s87, s63\n"
            "s_add_u32 s62, s87, 768\n"
            "s_movrels_b32 s92, s67\n"
            "s_movrels_b32 s93, s77\n"
            "v_mfma_f32_32x32x16_bf16 v[32:47], v[104:107], v[172:175], v[32:47]\n"
            "s_setprio 0\n"
            "s_waitcnt lgkmcnt(3)\n"
            "s_barrier\n"
            "s_waitcnt lgkmcnt(2)\n"
            "v_mfma_f32_16x16x32_bf16 v[96:99], v[188:191], v[112:115], v[96:99]\n"
            "ds_read_b128 v[16:19], v16\n"
            "s_add_u32 s95, s87, 98304\n"
            "s_add_u32 s94, s62, 98304\n"
            "s_waitcnt lgkmcnt(1)\n"
            "v_mfma_f32_16x16x32_bf16 v[100:103], v[188:191], v[116:119], v[100:103]\n"
            "v_mfma_f32_32x32x16_bf16 v[0:15], v[152:155], v[108:111], v[0:15]\n"
            "v_mfma_f32_32x32x16_bf16 v[32:47], v[108:111], v[176:179], v[32:47]\n"
            "s_waitcnt lgkmcnt(0)\n"
            "v_or3_b32 v16, v16, v17, v18\n"
            "v_or_b32 v16, v16, v19\n"
            "v_cmp_ne_u32 vcc, 0, v16\n"
            "s_cmp_lt_u32 s48, 7\n"
            "s_cbranch_scc1 LP3_%=\n"
            "s_setprio 3\n"
            "v_mfma_f32_32x32x16_bf16 v[16:31], v[84:87], v[80:83], v[192:207]\n"
            "ds_read_b128 v[104:107], v130\n"
            "ds_read_b128 v[108:111], v128\n"
            "v_cvt_pk_bf16_f32 v100, v100, v101\n"
            "v_cvt_pk_bf16_f32 v101, v102, v103\n"
            "v_cvt_pk_bf16_f32 v102, v96, v97\n"
            "v_cvt_pk_bf16_f32 v103, v98, v99\n"
            "v_cvt_pk_bf16_f32 v96, v8, v9\n"
            "v_mfma_f32_32x32x8_bf16 v[16:31], v[90:91], v[88:89], v[16:31]\n"
            "ds_read_b128 v[112:115], v128 offset:1024\n"
            "ds_read_b128 v[116:119], v128 offset:2048\n"
            "s_mov_b64 exec, s[60:61]\n"
            "v_cvt_pk_bf16_f32 v72, v32, v33\n"
            "v_cvt_pk_bf16_f32 v73, v34, v35\n"
            "v_cvt_pk_bf16_f32 v74, v36, v37\n"
            "v_cvt_pk_bf16_f32 v75, v38, v39\n"
            "s_mov_b64 exec, -1\n"
            "s_mov_b64 exec, s[60:61]\n"
            "v_cvt_pk_bf16_f32 v76, v40, v41\n"
            "v_cvt_pk_bf16_f32 v77, v42, v43\n"
            "v_cvt_pk_bf16_f32 v78, v44, v45\n"
            "v_cvt_pk_bf16_f32 v79, v46, v47\n"
            "s_mov_b64 exec, -1\n"
            "v_permlane16_swap_b32 v102, v100\n"
            "v_permlane16_swap_b32 v103, v101\n"
            "v_cvt_pk_bf16_f32 v97, v10, v11\n"
            "v_cvt_pk_bf16_f32 v98, v12, v13\n"
            "v_cvt_pk_bf16_f32 v99, v14, v15\n"
            "v_cvt_pk_bf16_f32 v92, v0, v1\n"
            "v_cvt_pk_bf16_f32 v93, v2, v3\n"
            "v_cvt_pk_bf16_f32 v94, v4, v5\n"
            "s_setprio 2\n"
            "v_cvt_pk_bf16_f32 v95, v6, v7\n"
            "s_cbranch_vccnz LM4_%=\n"
            "LM4_%=_r:\n"
            "v_mfma_f32_32x32x16_bf16 v[0:15], v[96:99], v[80:83], v[208:223]\n"
            "ds_read_b128 v[124:127], v130 offset:64\n"
            "v_exp_f32 v16, v16\n"
            "v_exp_f32 v17, v17\n"
            "v_exp_f32 v18, v18\n"
            "v_mfma_f32_32x32x8_bf16 v[0:15], v[100:101], v[88:89], v[0:15]\n"
            "v_exp_f32 v19, v19\n"
            "v_exp_f32 v20, v20\n"
            "v_exp_f32 v21, v21\n"
            "v_mfma_f32_32x32x16_bf16 v[32:47], v[84:87], v[92:95], v[224:239]\n"
            "v_exp_f32 v22, v22\n"
            "v_exp_f32 v23, v23\n"
            "s_nop 5\n"
            "v_exp_f32 v0, v0\n"
            "v_mfma_f32_32x32x16_bf16 v[48:63], v[96:99], v[92:95], v[240:255]\n"
            "v_exp_f32 v1, v1\n"
            "v_exp_f32 v2, v2\n"
            "v_exp_f32 v3, v3\n"
            "s_setprio 1\n"
            "v_mfma_f32_32x32x8_bf16 v[32:47], v[90:91], v[102:103], v[32:47]\n"
            "v_exp_f32 v4, v4\n"
            "v_exp_f32 v5, v5\n"
            "v_exp_f32 v6, v6\n"
            "v_mfma_f32_32x32x8_bf16 v[48:63], v[100:101], v[102:103], v[48:63]\n"
            "LM4_%=_j:\n"
            "v_exp_f32 v7, v7\n"
            "v_cvt_pk_bf16_f32 v0, v0, v1\n"
            "v_exp_f32 v8, v8\n"
            "v_cvt_pk_bf16_f32 v1, v2, v3\n"
            "s_waitcnt lgkmcnt(3)\n"
            "v_mfma_f32_16x16x32_bf16 v[80:83], v[104:107], v[108:111], 0\n"
            "ds_read_b128 v[108:111], v128 offset:3072\n"
            "v_exp_f32 v9, v9\n"
            "s_waitcnt lgkmcnt(3)\n"
            "v_mfma_f32_16x16x32_bf16 v[84:87], v[104:107], v[112:115], 0\n"
            "ds_read_b128 v[112:115], v128 offset:4096\n"
            "v_exp_f32 v10, v10\n"
            "s_waitcnt lgkmcnt(3)\n"
            "v_mfma_f32_16x16x32_bf16 v[88:91], v[104:107], v[116:119], 0\n"
            "ds_read_b128 v[116:119], v128 offset:5120\n"
            "v_exp_f32 v11, v11\n"
            "v_exp_f32 v12, v12\n"
            "v_exp_f32 v13, v13\n"
            "s_setprio 0\n"
            "v_cvt_pk_bf16_f32 v2, v4, v5\n"
            "s_waitcnt lgkmcnt(2)\n"
            "v_mfma_f32_16x16x32_bf16 v[92:95], v[104:107], v[108:111], 0\n"
            "ds_read_b128 v[108:111], v128 offset:6144\n"
            "v_exp_f32 v14, v14\n"
            "s_waitcnt lgkmcnt(2)\n"
            "v_mfma_f32_16x16x32_bf16 v[96:99], v[104:107], v[112:115], 0\n"
            "ds_read_b128 v[112:115], v128 offset:7168\n"
            "v_exp_f32 v15, v15\n"
            "s_waitcnt lgkmcnt(2)\n"
            "v_mfma_f32_16x16x32_bf16 v[100:103], v[104:107], v[116:119], 0\n"
            "ds_read_b128 v[116:119], v128 offset:8192\n"
            "ds_read_b128 v[104:107], v130 offset:128\n"
            "v_exp_f32 v24, v24\n"
            "v_exp_f32 v25, v25\n"
            "v_exp_f32 v26, v26\n"
            "s_waitcnt lgkmcnt(3)\n"
            "v_mfma_f32_16x16x32_bf16 v[80:83], v[124:127], v[108:111], v[80:83]\n"
            "ds_read_b128 v[108:111], v128 offset:9216\n"
            "v_exp_f32 v27, v27\n"
            "s_waitcnt lgkmcnt(3)\n"
            "v_mfma_f32_16x16x32_bf16 v[84:87], v[124:127], v[112:115], v[84:87]\n"
            "ds_read_b128 v[112:115], v128 offset:10240\n"
            "v_exp_f32 v28, v28\n"
            "s_waitcnt lgkmcnt(3)\n"
            "v_mfma_f32_16x16x32_bf16 v[88:91], v[124:127], v[116:119], v[88:91]\n"
            "ds_read_b128 v[116:119], v128 offset:11264\n"
            "v_exp_f32 v29, v29\n"
            "v_exp_f32 v30, v30\n"
            "v_exp_f32 v31, v31\n"
            "s_setprio 3\n"
            "v_cvt_pk_bf16_f32 v3, v6, v7\n"
            "s_waitcnt lgkmcnt(2)\n"
            "v_mfma_f32_16x16x32_bf16 v[92:95], v[124:127], v[108:111], v[92:95]\n"
            "ds_read_b128 v[108:111], v128 offset:12288\n"
            "v_cvt_pk_bf16_f32 v4, v8, v9\n"
            "v_cvt_pk_bf16_f32 v5, v10, v11\n"
            "s_waitcnt lgkmcnt(2)\n"
            "v_mfma_f32_16x16x32_bf16 v[96:99], v[124:127], v[112:115], v[96:99]\n"
            "ds_read_b128 v[112:115], v128 offset:13312\n"
            "v_cvt_pk_bf16_f32 v6, v12, v13\n"
            "v_cvt_pk_bf16_f32 v7, v14, v15\n"
            "s_waitcnt lgkmcnt(2)\n"
            "v_mfma_f32_16x16x32_bf16 v[100:103], v[124:127], v[116:119], v[100:103]\n"
            "ds_read_b128 v[116:119], v128 offset:14336\n"
            "ds_read_b128 v[124:127], v128 offset:15360\n"
            "v_cvt_pk_bf16_f32 v8, v16, v17\n"
            "v_cvt_pk_bf16_f32 v9, v18, v19\n"
            "v_cvt_pk_bf16_f32 v10, v20, v21\n"
            "v_cvt_pk_bf16_f32 v11, v22, v23\n"
            "v_cvt_pk_bf16_f32 v12, v24, v25\n"
            "v_cvt_pk_bf16_f32 v13, v26, v27\n"
            "s_waitcnt lgkmcnt(3)\n"
            "v_mfma_f32_16x16x32_bf16 v[80:83], v[104:107], v[108:111], v[80:83]\n"
            "ds_read_b128 v[108:111], v128 offset:16384\n"
            "v_cvt_pk_bf16_f32 v14, v28, v29\n"
            "v_cvt_pk_bf16_f32 v15, v30, v31\n"
            "s_waitcnt lgkmcnt(3)\n"
            "v_mfma_f32_16x16x32_bf16 v[84:87], v[104:107], v[112:115], v[84:87]\n"
            "ds_read_b128 v[112:115], v128 offset:17408\n"
            "v_exp_f32 v32, v32\n"
            "s_waitcnt lgkmcnt(3)\n"
            "v_mfma_f32_16x16x32_bf16 v[88:91], v[104:107], v[116:119], v[88:91]\n"
            "s_barrier\n"
            "s_setprio 2\n"
            "s_waitcnt lgkmcnt(2)\n"
            "v_mfma_f32_16x16x32_bf16 v[92:95], v[104:107], v[124:127], v[92:95]\n"
            "v_exp_f32 v33, v33\n"
            "s_waitcnt lgkmcnt(1)\n"
            "v_mfma_f32_16x16x32_bf16 v[96:99], v[104:107], v[108:111], v[96:99]\n"
            "v_exp_f32 v34, v34\n"
            "s_waitcnt lgkmcnt(0)\n"
            "v_mfma_f32_16x16x32_bf16 v[100:103], v[104:107], v[112:115], v[100:103]\n"
            "v_exp_f32 v35, v35\n"
            "v_mfma_f32_32x32x16_bf16 v[16:31], v[72:75], v[0:3], 0\n"
            "ds_read_b128 v[0:3], v128\n"
            "v_exp_f32 v36, v36\n"
            "v_exp_f32 v37, v37\n"
            "v_exp_f32 v38, v38\n"
            "v_mfma_f32_32x32x16_bf16 v[16:31], v[76:79], v[4:7], v[16:31]\n"
            "ds_read_b128 v[4:7], v128 offset:1024\n"
            "v_exp_f32 v39, v39\n"
            "v_cvt_pk_bf16_f32 v32, v32, v33\n"
            "s_setprio 1\n"
            "v_exp_f32 v40, v40\n"
            "v_cvt_pk_bf16_f32 v33, v34, v35\n"
            "v_mfma_f32_32x32x16_bf16 v[16:31], v[64:67], v[8:11], v[16:31]\n"
            "ds_read_b128 v[8:11], v128 offset:2048\n"
            "v_exp_f32 v41, v41\n"
            "v_exp_f32 v42, v42\n"
            "v_exp_f32 v43, v43\n"
            "v_mfma_f32_32x32x16_bf16 v[16:31], v[68:71], v[12:15], v[16:31]\n"
            "ds_read_b128 v[12:15], v128 offset:3072\n"
            "v_exp_f32 v44, v44\n"
            "v_exp_f32 v45, v45\n"
            "v_exp_f32 v46, v46\n"
            "v_exp_f32 v47, v47\n"
            "v_exp_f32 v48, v48\n"
            "v_exp_f32 v49, v49\n"
            "v_exp_f32 v50, v50\n"
            "v_exp_f32 v51, v51\n"
            "v_exp_f32 v52, v52\n"
            "v_exp_f32 v53, v53\n"
            "s_setprio 0\n"
            "v_exp_f32 v54, v54\n"
            "v_exp_f32 v55, v55\n"
            "v_exp_f32 v56, v56\n"
            "v_exp_f32 v57, v57\n"
            "v_exp_f32 v58, v58\n"
            "v_exp_f32 v59, v59\n"
            "v_exp_f32 v60, v60\n"
            "v_exp_f32 v61, v61\n"
            "v_exp_f32 v62, v62\n"
            "v_exp_f32 v63, v63\n"
            "v_cvt_pk_bf16_f32 v34, v36, v37\n"
            "v_cvt_pk_bf16_f32 v35, v38, v39\n"
            "v_cvt_pk_bf16_f32 v36, v40, v41\n"
            "v_cvt_pk_bf16_f32 v37, v42, v43\n"
            "v_cvt_pk_bf16_f32 v38, v44, v45\n"
            "v_cvt_pk_bf16_f32 v39, v46, v47\n"
            "v_cvt_pk_bf16_f32 v40, v48, v49\n"
            "v_cvt_pk_bf16_f32 v41, v50, v51\n"
            "v_cvt_pk_bf16_f32 v42, v52, v53\n"
            "v_cvt_pk_bf16_f32 v43, v54, v55\n"
            "v_cvt_pk_bf16_f32 v44, v56, v57\n"
            "v_cvt_pk_bf16_f32 v45, v58, v59\n"
            "v_cvt_pk_bf16_f32 v46, v60, v61\n"
            "s_setprio 3\n"
            "v_cvt_pk_bf16_f32 v47, v62, v63\n"
            "v_mfma_f32_32x32x16_bf16 v[48:63], v[64:67], v[32:35], 0\n"
            "ds_read_b128 v[32:35], v128 offset:6144\n"
            "v_cvt_pk_bf16_f32 v126, v96, v100\n"
            "v_cvt_pk_bf16_f32 v124, v80, v84\n"
            "v_cvt_pk_bf16_f32 v125, v88, v92\n"
            "v_rcp_f32 v29, v28\n"
            "v_cvt_pk_bf16_f32 v118, v97, v101\n"
            "v_mfma_f32_32x32x16_bf16 v[48:63], v[68:71], v[36:39], v[48:63]\n"
            "buffer_store_dwordx3 v[124:126], v129, s[44:47], s87 offen sc1\n"
            "v_mul_f32 v16, v16, v29\n"
            "v_mul_f32 v17, v17, v29\n"
            "v_cvt_pk_bf16_f32 v16, v16, v17\n"
            "v_mul_f32 v18, v18, v29\n"
            "v_mul_f32 v19, v19, v29\n"
            "v_mfma_f32_32x32x16_bf16 v[48:63], v[72:75], v[40:43], v[48:63]\n"
            "ds_read_b128 v[124:127], v128 offset:5120\n"
            "ds_read_b128 v[36:39], v128 offset:7168\n"
            "v_cvt_pk_bf16_f32 v17, v18, v19\n"
            "v_mul_f32 v20, v20, v29\n"
            "v_mul_f32 v21, v21, v29\n"
            "v_cvt_pk_bf16_f32 v18, v20, v21\n"
            "v_mul_f32 v22, v22, v29\n"
            "v_mfma_f32_32x32x16_bf16 v[48:63], v[76:79], v[44:47], v[48:63]\n"
            "ds_write_b64 v123, v[16:17]\n"
            "v_mul_f32 v23, v23, v29\n"
            "v_mul_f32 v24, v24, v29\n"
            "v_mul_f32 v25, v25, v29\n"
            "v_mul_f32 v26, v26, v29\n"
            "v_mul_f32 v27, v27, v29\n"
            "v_cvt_pk_bf16_f32 v19, v22, v23\n"
            "s_setprio 2\n"
            "v_cvt_pk_bf16_f32 v20, v24, v25\n"
            "v_cvt_pk_bf16_f32 v21, v26, v27\n"
            "ds_write_b64 v123, v[18:19] offset:64\n"
            "ds_write_b64 v123, v[20:21] offset:128\n"
            "v_cvt_pk_bf16_f32 v116, v81, v85\n"
            "v_cvt_pk_bf16_f32 v117, v89, v93\n"
            "v_rcp_f32 v61, v60\n"
            "buffer_store_dwordx3 v[116:118], v129, s[44:47], s62 offen sc1\n"
            "v_mul_f32 v48, v48, v61\n"
            "v_mul_f32 v49, v49, v61\n"
            "v_cvt_pk_bf16_f32 v48, v48, v49\n"
            "v_mul_f32 v50, v50, v61\n"
            "v_mul_f32 v51, v51, v61\n"
            "v_cvt_pk_bf16_f32 v49, v50, v51\n"
            "v_mul_f32 v52, v52, v61\n"
            "v_mul_f32 v53, v53, v61\n"
            "v_cvt_pk_bf16_f32 v50, v52, v53\n"
            "v_mul_f32 v54, v54, v61\n"
            "v_mul_f32 v55, v55, v61\n"
            "v_mul_f32 v56, v56, v61\n"
            "v_mul_f32 v57, v57, v61\n"
            "v_mul_f32 v58, v58, v61\n"
            "v_mul_f32 v59, v59, v61\n"
            "v_cvt_pk_bf16_f32 v51, v54, v55\n"
            "v_cvt_pk_bf16_f32 v52, v56, v57\n"
            "v_cvt_pk_bf16_f32 v53, v58, v59\n"
            "ds_write_b64 v123, v[48:49] offset:6144\n"
            "ds_write_b64 v123, v[50:51] offset:6208\n"
            "ds_write_b64 v123, v[52:53] offset:6272\n"
            "v_cvt_pk_bf16_f32 v98, v98, v102\n"
            "v_cvt_pk_bf16_f32 v96, v82, v86\n"
            "s_setprio 1\n"
            "v_cvt_pk_bf16_f32 v97, v90, v94\n"
            "buffer_store_dwordx3 v[96:98], v129, s[44:47], s95 offen sc1\n"
            "v_cvt_pk_bf16_f32 v100, v83, v87\n"
            "ds_read_b128 v[116:119], v128 offset:4096\n"
            "v_cvt_pk_bf16_f32 v101, v91, v95\n"
            "v_cvt_pk_bf16_f32 v102, v99, v103\n"
            "ds_read_b128 v[40:43], v128 offset:8192\n"
            "ds_read_b128 v[44:47], v128 offset:9216\n"
            "s_add_u32 s48, s48, 1\n"
            "s_mov_b32 m0, s48\n"
            "v_max_f32 v30, v28, v29\n"
            "s_movrels_b32 s87, s63\n"
            "s_waitcnt lgkmcnt(3)\n"
            "s_barrier\n"
            "ds_read_b128 v[104:107], v130\n"
            "ds_read_b128 v[108:111], v130 offset:64\n"
            "buffer_store_dwordx3 v[100:102], v129, s[44:47], s94 offen sc1\n"
            "ds_read_b128 v[112:115], v130 offset:128\n"
            "s_add_u32 s62, s87, 768\n"
            "s_add_u32 s95, s87, 98304\n"
            "v_cmp_nge_f32_e64 s[84:85], s86, v30\n"
            "s_setprio 0\n"
            "v_max_f32 v62, v60, v61\n"
            "s_add_u32 s94, s62, 98304\n"
            "v_cmp_nge_f32 vcc, s86, v62\n"
            "s_or_b64 vcc, vcc, s[84:85]\n"
            "s_addc_u32 s97, s97, s97\n"
            "s_waitcnt lgkmcnt(2)\n"
            "v_mfma_f32_16x16x32_bf16 v[80:83], v[104:107], v[0:3], 0\n"
            "ds_read_b128 v[0:3], v128 offset:10240\n"
            "v_mfma_f32_16x16x32_bf16 v[84:87], v[104:107], v[4:7], 0\n"
            "ds_read_b128 v[4:7], v128 offset:11264\n"
            "v_mfma_f32_16x16x32_bf16 v[88:91], v[104:107], v[8:11], 0\n"
            "ds_read_b128 v[8:11], v128 offset:12288\n"
            "v_mfma_f32_16x16x32_bf16 v[92:95], v[104:107], v[12:15], 0\n"
            "ds_read_b128 v[12:15], v128 offset:13312\n"
            "v_mfma_f32_16x16x32_bf16 v[96:99], v[104:107], v[116:119], 0\n"
            "ds_read_b128 v[116:119], v128 offset:14336\n"
            "v_mfma_f32_16x16x32_bf16 v[100:103], v[104:107], v[124:127], 0\n"
            "ds_read_b128 v[124:127], v128 offset:15360\n"
            "ds_read_b128 v[104:107], v128 offset:16384\n"
            "s_waitcnt lgkmcnt(8)\n"
            "v_mfma_f32_16x16x32_bf16 v[80:83], v[108:111], v[32:35], v[80:83]\n"
            "s_setprio 3\n"
            "ds_read_b128 v[32:35], v128 offset:17408\n"
            "v_mfma_f32_16x16x32_bf16 v[84:87], v[108:111], v[36:39], v[84:87]\n"
            "v_mfma_f32_16x16x32_bf16 v[88:91], v[108:111], v[40:43], v[88:91]\n"
            "v_mfma_f32_16x16x32_bf16 v[92:95], v[108:111], v[44:47], v[92:95]\n"
            "s_waitcnt lgkmcnt(7)\n"
            "v_mfma_f32_16x16x32_bf16 v[96:99], v[108:111], v[0:3], v[96:99]\n"
            "s_waitcnt lgkmcnt(6)\n"
            "v_mfma_f32_16x16x32_bf16 v[100:103], v[108:111], v[4:7], v[100:103]\n"
            "s_waitcnt lgkmcnt(5)\n"
            "v_mfma_f32_16x16x32_bf16 v[80:83], v[112:115], v[8:11], v[80:83]\n"
            "s_waitcnt lgkmcnt(4)\n"
            "v_mfma_f32_16x16x32_bf16 v[84:87], v[112:115], v[12:15], v[84:87]\n"
            "s_waitcnt lgkmcnt(3)\n"
            "v_mfma_f32_16x16x32_bf16 v[88:91], v[112:115], v[116:119], v[88:91]\n"
            "s_setprio 2\n"
            "s_waitcnt lgkmcnt(2)\n"
            "v_mfma_f32_16x16x32_bf16 v[92:95], v[112:115], v[124:127], v[92:95]\n"
            "s_nop 2\n"
            "v_cvt_pk_bf16_f32 v16, v80, v84\n"
            "v_cvt_pk_bf16_f32 v20, v81, v85\n"
            "s_waitcnt lgkmcnt(1)\n"
            "v_mfma_f32_16x16x32_bf16 v[96:99], v[112:115], v[104:107], v[96:99]\n"
            "s_waitcnt lgkmcnt(0)\n"
            "v_mfma_f32_16x16x32_bf16 v[100:103], v[112:115], v[32:35], v[100:103]\n"
            "v_cvt_pk_bf16_f32 v17, v88, v92\n"
            "v_cvt_pk_bf16_f32 v21, v89, v93\n"
            "s_nop 5\n"
            "v_cvt_pk_bf16_f32 v18, v96, v100\n"
            "buffer_store_dwordx3 v[16:18], v129, s[44:47], s87 offen sc1\n"
            "v_cvt_pk_bf16_f32 v22, v97, v101\n"
            "v_cvt_pk_bf16_f32 v98, v98, v102\n"
            "v_cvt_pk_bf16_f32 v96, v82, v86\n"
            "v_cvt_pk_bf16_f32 v97, v90, v94\n"
            "v_cvt_pk_bf16_f32 v102, v99, v103\n"
            "v_cvt_pk_bf16_f32 v100, v83, v87\n"
            "v_cvt_pk_bf16_f32 v101, v91, v95\n"
            "s_setprio 1\n"
            "buffer_store_dwordx3 v[20:22], v129, s[44:47], s62 offen sc1\n"
            "s_setprio 0\n"
            "buffer_store_dwordx3 v[96:98], v129, s[44:47], s95 offen sc1\n"
            "buffer_store_dwordx3 v[100:102], v129, s[44:47], s94 offen sc1\n"
            "s_setprio 0\n"
            "s_branch LX5_%=\n"
            "LM1_%=:\n"
            "s_sub_u32 s96, s56, s57\n"
            "v_and_b32 v60, 0x1f0, v128\n"
            "s_lshl_b32 s96, s96, 9\n"
            "v_lshrrev_b32 v61, 6, v128\n"
            "s_sub_u32 s96, s54, s96\n"
            "v_and_b32 v61, 8, v61\n"
            "v_lshl_add_u32 v61, v60, 3, v61\n"
            "v_lshrrev_b32 v60, 1, v60\n"
            "v_and_b32 v60, 0x30, v60\n"
            "v_add3_u32 v56, v61, v60, s96\n"
            "v_xor_b32 v57, 16, v56\n"
            "v_xor_b32 v58, 32, v56\n"
            "v_xor_b32 v59, 48, v56\n"
            "s_mov_b32 s96, 0x3fb8aa3b\n"
            "ds_read_b64 v[2:3], v56 offset:64\n"
            "ds_read_b64 v[6:7], v57 offset:64\n"
            "ds_read_b64 v[10:11], v58 offset:64\n"
            "ds_read_b64 v[14:15], v59 offset:64\n"
            "ds_read_b64 v[34:35], v56 offset:4096\n"
            "ds_read_b64 v[38:39], v57 offset:4096\n"
            "ds_read_b64 v[42:43], v58 offset:4096\n"
            "ds_read_b64 v[46:47], v59 offset:4096\n"
            "ds_read_b64 v[48:49], v56\n"
            "ds_read_b64 v[50:51], v57\n"
            "ds_read_b64 v[52:53], v58\n"
            "ds_read_b64 v[54:55], v59\n"
            "s_waitcnt lgkmcnt(8)\n"
            "v_lshlrev_b32 v0, 16, v2\n"
            "v_and_b32 v1, 0xffff0000, v2\n"
            "v_lshlrev_b32 v2, 16, v3\n"
            "v_and_b32 v3, 0xffff0000, v3\n"
            "v_fma_f32 v0, v0, s96, v208\n"
            "v_fma_f32 v1, v1, s96, v209\n"
            "v_fma_f32 v2, v2, s96, v210\n"
            "v_fma_f32 v3, v3, s96, v211\n"
            "v_lshlrev_b32 v4, 16, v6\n"
            "v_and_b32 v5, 0xffff0000, v6\n"
            "v_lshlrev_b32 v6, 16, v7\n"
            "v_and_b32 v7, 0xffff0000, v7\n"
            "v_fma_f32 v4, v4, s96, v212\n"
            "v_fma_f32 v5, v5, s96, v213\n"
            "v_fma_f32 v6, v6, s96, v214\n"
            "v_fma_f32 v7, v7, s96, v215\n"
            "v_lshlrev_b32 v8, 16, v10\n"
            "v_and_b32 v9, 0xffff0000, v10\n"
            "v_lshlrev_b32 v10, 16, v11\n"
            "v_and_b32 v11, 0xffff0000, v11\n"
            "v_fma_f32 v8, v8, s96, v216\n"
            "v_fma_f32 v9, v9, s96, v217\n"
            "v_fma_f32 v10, v10, s96, v218\n"
            "v_fma_f32 v11, v11, s96, v219\n"
            "v_lshlrev_b32 v12, 16, v14\n"
            "v_and_b32 v13, 0xffff0000, v14\n"
            "v_lshlrev_b32 v14, 16, v15\n"
            "v_and_b32 v15, 0xffff0000, v15\n"
            "v_fma_f32 v12, v12, s96, v220\n"
            "v_fma_f32 v13, v13, s96, v221\n"
            "v_fma_f32 v14, v14, s96, v222\n"
            "v_fma_f32 v15, v15, s96, v223\n"
            "s_waitcnt lgkmcnt(0)\n"
            "v_lshlrev_b32 v60, 16, v48\n"
            "v_fmac_f32 v16, s96, v60\n"
            "v_and_b32 v61, 0xffff0000, v48\n"
            "v_fmac_f32 v17, s96, v61\n"
            "v_lshlrev_b32 v62, 16, v49\n"
            "v_fmac_f32 v18, s96, v62\n"
            "v_and_b32 v63, 0xffff0000, v49\n"
            "v_fmac_f32 v19, s96, v63\n"
            "v_lshlrev_b32 v60, 16, v50\n"
            "v_fmac_f32 v20, s96, v60\n"
            "v_and_b32 v61, 0xffff0000, v50\n"
            "v_fmac_f32 v21, s96, v61\n"
            "v_lshlrev_b32 v62, 16, v51\n"
            "v_fmac_f32 v22, s96, v62\n"
            "v_and_b32 v63, 0xffff0000, v51\n"
            "v_fmac_f32 v23, s96, v63\n"
            "v_lshlrev_b32 v60, 16, v52\n"
            "v_fmac_f32 v24, s96, v60\n"
            "v_and_b32 v61, 0xffff0000, v52\n"
            "v_fmac_f32 v25, s96, v61\n"
            "v_lshlrev_b32 v62, 16, v53\n"
            "v_fmac_f32 v26, s96, v62\n"
            "v_and_b32 v63, 0xffff0000, v53\n"
            "v_fmac_f32 v27, s96, v63\n"
            "v_lshlrev_b32 v60, 16, v54\n"
            "v_fmac_f32 v28, s96, v60\n"
            "v_and_b32 v61, 0xffff0000, v54\n"
            "v_fmac_f32 v29, s96, v61\n"
            "v_lshlrev_b32 v62, 16, v55\n"
            "v_fmac_f32 v30, s96, v62\n"
            "v_and_b32 v63, 0xffff0000, v55\n"
            "v_fmac_f32 v31, s96, v63\n"
            "ds_read_b64 v[50:51], v56 offset:4160\n"
            "ds_read_b64 v[54:55], v57 offset:4160\n"
            "ds_read_b64 v[62:63], v59 offset:4160\n"
            "ds_read_b64 v[58:59], v58 offset:4160\n"
            "v_lshlrev_b32 v32, 16, v34\n"
            "v_and_b32 v33, 0xffff0000, v34\n"
            "v_lshlrev_b32 v34, 16, v35\n"
            "v_and_b32 v35, 0xffff0000, v35\n"
            "v_fma_f32 v32, v32, s96, v224\n"
            "v_fma_f32 v33, v33, s96, v225\n"
            "v_fma_f32 v34, v34, s96, v226\n"
            "v_fma_f32 v35, v35, s96, v227\n"
            "v_lshlrev_b32 v36, 16, v38\n"
            "v_and_b32 v37, 0xffff0000, v38\n"
            "v_lshlrev_b32 v38, 16, v39\n"
            "v_and_b32 v39, 0xffff0000, v39\n"
            "v_fma_f32 v36, v36, s96, v228\n"
            "v_fma_f32 v37, v37, s96, v229\n"
            "v_fma_f32 v38, v38, s96, v230\n"
            "v_fma_f32 v39, v39, s96, v231\n"
            "v_lshlrev_b32 v40, 16, v42\n"
            "v_and_b32 v41, 0xffff0000, v42\n"
            "v_lshlrev_b32 v42, 16, v43\n"
            "v_and_b32 v43, 0xffff0000, v43\n"
            "v_fma_f32 v40, v40, s96, v232\n"
            "v_fma_f32 v41, v41, s96, v233\n"
            "v_fma_f32 v42, v42, s96, v234\n"
            "v_fma_f32 v43, v43, s96, v235\n"
            "v_lshlrev_b32 v44, 16, v46\n"
            "v_and_b32 v45, 0xffff0000, v46\n"
            "v_lshlrev_b32 v46, 16, v47\n"
            "v_and_b32 v47, 0xffff0000, v47\n"
            "v_fma_f32 v44, v44, s96, v236\n"
            "v_fma_f32 v45, v45, s96, v237\n"
            "v_fma_f32 v46, v46, s96, v238\n"
            "v_fma_f32 v47, v47, s96, v239\n"
            "s_waitcnt lgkmcnt(0)\n"
            "v_lshlrev_b32 v48, 16, v50\n"
            "v_and_b32 v49, 0xffff0000, v50\n"
            "v_lshlrev_b32 v50, 16, v51\n"
            "v_and_b32 v51, 0xffff0000, v51\n"
            "v_fma_f32 v48, v48, s96, v240\n"
            "v_fma_f32 v49, v49, s96, v241\n"
            "v_fma_f32 v50, v50, s96, v242\n"
            "v_fma_f32 v51, v51, s96, v243\n"
            "v_lshlrev_b32 v52, 16, v54\n"
            "v_and_b32 v53, 0xffff0000, v54\n"
            "v_lshlrev_b32 v54, 16, v55\n"
            "v_and_b32 v55, 0xffff0000, v55\n"
            "v_fma_f32 v52, v52, s96, v244\n"
            "v_fma_f32 v53, v53, s96, v245\n"
            "v_fma_f32 v54, v54, s96, v246\n"
            "v_fma_f32 v55, v55, s96, v247\n"
            "v_lshlrev_b32 v56, 16, v58\n"
            "v_and_b32 v57, 0xffff0000, v58\n"
            "v_lshlrev_b32 v58, 16, v59\n"
            "v_and_b32 v59, 0xffff0000, v59\n"
            "v_fma_f32 v56, v56, s96, v248\n"
            "v_fma_f32 v57, v57, s96, v249\n"
            "v_fma_f32 v58, v58, s96, v250\n"
            "v_fma_f32 v59, v59, s96, v251\n"
            "v_lshlrev_b32 v60, 16, v62\n"
            "v_and_b32 v61, 0xffff0000, v62\n"
            "v_lshlrev_b32 v62, 16, v63\n"
            "v_and_b32 v63, 0xffff0000, v63\n"
            "v_fma_f32 v60, v60, s96, v252\n"
            "v_fma_f32 v61, v61, s96, v253\n"
            "v_fma_f32 v62, v62, s96, v254\n"
            "v_fma_f32 v63, v63, s96, v255\n"
            "s_nop 1\n"
            "v_mfma_f32_32x32x16_bf16 v[0:15], v[96:99], v[80:83], v[0:15]\n"
            "v_exp_f32 v16, v16\n"
            "v_exp_f32 v17, v17\n"
            "v_exp_f32 v18, v18\n"
            "v_mfma_f32_32x32x8_bf16 v[0:15], v[100:101], v[88:89], v[0:15]\n"
            "v_exp_f32 v19, v19\n"
            "v_exp_f32 v20, v20\n"
            "v_exp_f32 v21, v21\n"
            "v_mfma_f32_32x32x16_bf16 v[32:47], v[84:87], v[92:95], v[32:47]\n"
            "v_exp_f32 v22, v22\n"
            "v_exp_f32 v23, v23\n"
            "s_nop 5\n"
            "v_exp_f32 v0, v0\n"
            "v_mfma_f32_32x32x16_bf16 v[48:63], v[96:99], v[92:95], v[48:63]\n"
            "s_setprio 1\n"
            "v_exp_f32 v1, v1\n"
            "v_exp_f32 v2, v2\n"
            "v_exp_f32 v3, v3\n"
            "v_mfma_f32_32x32x8_bf16 v[32:47], v[90:91], v[102:103], v[32:47]\n"
            "v_exp_f32 v4, v4\n"
            "v_exp_f32 v5, v5\n"
            "v_exp_f32 v6, v6\n"
            "v_mfma_f32_32x32x8_bf16 v[48:63], v[100:101], v[102:103], v[48:63]\n"
            "s_branch LM1_%=_j\n"
            "LM2_%=:\n"
            "s_sub_u32 s96, s56, s57\n"
            "v_and_b32 v60, 0x1f0, v128\n"
            "s_lshl_b32 s96, s96, 9\n"
            "v_lshrrev_b32 v61, 6, v128\n"
            "s_sub_u32 s96, s54, s96\n"
            "v_and_b32 v61, 8, v61\n"
            "v_lshl_add_u32 v61, v60, 3, v61\n"
            "v_lshrrev_b32 v60, 1, v60\n"
            "v_and_b32 v60, 0x30, v60\n"
            "v_add3_u32 v56, v61, v60, s96\n"
            "v_xor_b32 v57, 16, v56\n"
            "v_xor_b32 v58, 32, v56\n"
            "v_xor_b32 v59, 48, v56\n"
            "s_mov_b32 s96, 0x3fb8aa3b\n"
            "ds_read_b64 v[2:3], v56 offset:64\n"
            "ds_read_b64 v[6:7], v57 offset:64\n"
            "ds_read_b64 v[10:11], v58 offset:64\n"
            "ds_read_b64 v[14:15], v59 offset:64\n"
            "ds_read_b64 v[34:35], v56 offset:4096\n"
            "ds_read_b64 v[38:39], v57 offset:4096\n"
            "ds_read_b64 v[42:43], v58 offset:4096\n"
            "ds_read_b64 v[46:47], v59 offset:4096\n"
            "ds_read_b64 v[48:49], v56\n"
            "ds_read_b64 v[50:51], v57\n"
            "ds_read_b64 v[52:53], v58\n"
            "ds_read_b64 v[54:55], v59\n"
            "s_waitcnt lgkmcnt(8)\n"
            "v_lshlrev_b32 v0, 16, v2\n"
            "v_and_b32 v1, 0xffff0000, v2\n"
            "v_lshlrev_b32 v2, 16, v3\n"
            "v_and_b32 v3, 0xffff0000, v3\n"
            "v_fma_f32 v0, v0, s96, v208\n"
            "v_fma_f32 v1, v1, s96, v209\n"
            "v_fma_f32 v2, v2, s96, v210\n"
            "v_fma_f32 v3, v3, s96, v211\n"
            "v_lshlrev_b32 v4, 16, v6\n"
            "v_and_b32 v5, 0xffff0000, v6\n"
            "v_lshlrev_b32 v6, 16, v7\n"
            "v_and_b32 v7, 0xffff0000, v7\n"
            "v_fma_f32 v4, v4, s96, v212\n"
            "v_fma_f32 v5, v5, s96, v213\n"
            "v_fma_f32 v6, v6, s96, v214\n"
            "v_fma_f32 v7, v7, s96, v215\n"
            "v_lshlrev_b32 v8, 16, v10\n"
            "v_and_b32 v9, 0xffff0000, v10\n"
            "v_lshlrev_b32 v10, 16, v11\n"
            "v_and_b32 v11, 0xffff0000, v11\n"
            "v_fma_f32 v8, v8, s96, v216\n"
            "v_fma_f32 v9, v9, s96, v217\n"
            "v_fma_f32 v10, v10, s96, v218\n"
            "v_fma_f32 v11, v11, s96, v219\n"
            "v_lshlrev_b32 v12, 16, v14\n"
            "v_and_b32 v13, 0xffff0000, v14\n"
            "v_lshlrev_b32 v14, 16, v15\n"
            "v_and_b32 v15, 0xffff0000, v15\n"
            "v_fma_f32 v12, v12, s96, v220\n"
            "v_fma_f32 v13, v13, s96, v221\n"
            "v_fma_f32 v14, v14, s96, v222\n"
            "v_fma_f32 v15, v15, s96, v223\n"
            "s_waitcnt lgkmcnt(0)\n"
            "v_lshlrev_b32 v60, 16, v48\n"
            "v_fmac_f32 v16, s96, v60\n"
            "v_and_b32 v61, 0xffff0000, v48\n"
            "v_fmac_f32 v17, s96, v61\n"
            "v_lshlrev_b32 v62, 16, v49\n"
            "v_fmac_f32 v18, s96, v62\n"
            "v_and_b32 v63, 0xffff0000, v49\n"
            "v_fmac_f32 v19, s96, v63\n"
            "v_lshlrev_b32 v60, 16, v50\n"
            "v_fmac_f32 v20, s96, v60\n"
            "v_and_b32 v61, 0xffff0000, v50\n"
            "v_fmac_f32 v21, s96, v61\n"
            "v_lshlrev_b32 v62, 16, v51\n"
            "v_fmac_f32 v22, s96, v62\n"
            "v_and_b32 v63, 0xffff0000, v51\n"
            "v_fmac_f32 v23, s96, v63\n"
            "v_lshlrev_b32 v60, 16, v52\n"
            "v_fmac_f32 v24, s96, v60\n"
            "v_and_b32 v61, 0xffff0000, v52\n"
            "v_fmac_f32 v25, s96, v61\n"
            "v_lshlrev_b32 v62, 16, v53\n"
            "v_fmac_f32 v26, s96, v62\n"
            "v_and_b32 v63, 0xffff0000, v53\n"
            "v_fmac_f32 v27, s96, v63\n"
            "v_lshlrev_b32 v60, 16, v54\n"
            "v_fmac_f32 v28, s96, v60\n"
            "v_and_b32 v61, 0xffff0000, v54\n"
            "v_fmac_f32 v29, s96, v61\n"
            "v_lshlrev_b32 v62, 16, v55\n"
            "v_fmac_f32 v30, s96, v62\n"
            "v_and_b32 v63, 0xffff0000, v55\n"
            "v_fmac_f32 v31, s96, v63\n"
            "ds_read_b64 v[50:51], v56 offset:4160\n"
            "ds_read_b64 v[54:55], v57 offset:4160\n"
            "ds_read_b64 v[62:63], v59 offset:4160\n"
            "ds_read_b64 v[58:59], v58 offset:4160\n"
            "v_lshlrev_b32 v32, 16, v34\n"
            "v_and_b32 v33, 0xffff0000, v34\n"
            "v_lshlrev_b32 v34, 16, v35\n"
            "v_and_b32 v35, 0xffff0000, v35\n"
            "v_fma_f32 v32, v32, s96, v224\n"
            "v_fma_f32 v33, v33, s96, v225\n"
            "v_fma_f32 v34, v34, s96, v226\n"
            "v_fma_f32 v35, v35, s96, v227\n"
            "v_lshlrev_b32 v36, 16, v38\n"
            "v_and_b32 v37, 0xffff0000, v38\n"
            "v_lshlrev_b32 v38, 16, v39\n"
            "v_and_b32 v39, 0xffff0000, v39\n"
            "v_fma_f32 v36, v36, s96, v228\n"
            "v_fma_f32 v37, v37, s96, v229\n"
            "v_fma_f32 v38, v38, s96, v230\n"
            "v_fma_f32 v39, v39, s96, v231\n"
            "v_lshlrev_b32 v40, 16, v42\n"
            "v_and_b32 v41, 0xffff0000, v42\n"
            "v_lshlrev_b32 v42, 16, v43\n"
            "v_and_b32 v43, 0xffff0000, v43\n"
            "v_fma_f32 v40, v40, s96, v232\n"
            "v_fma_f32 v41, v41, s96, v233\n"
            "v_fma_f32 v42, v42, s96, v234\n"
            "v_fma_f32 v43, v43, s96, v235\n"
            "v_lshlrev_b32 v44, 16, v46\n"
            "v_and_b32 v45, 0xffff0000, v46\n"
            "v_lshlrev_b32 v46, 16, v47\n"
            "v_and_b32 v47, 0xffff0000, v47\n"
            "v_fma_f32 v44, v44, s96, v236\n"
            "v_fma_f32 v45, v45, s96, v237\n"
            "v_fma_f32 v46, v46, s96, v238\n"
            "v_fma_f32 v47, v47, s96, v239\n"
            "s_waitcnt lgkmcnt(0)\n"
            "v_lshlrev_b32 v48, 16, v50\n"
            "v_and_b32 v49, 0xffff0000, v50\n"
            "v_lshlrev_b32 v50, 16, v51\n"
            "v_and_b32 v51, 0xffff0000, v51\n"
            "v_fma_f32 v48, v48, s96, v240\n"
            "v_fma_f32 v49, v49, s96, v241\n"
            "v_fma_f32 v50, v50, s96, v242\n"
            "v_fma_f32 v51, v51, s96, v243\n"
            "v_lshlrev_b32 v52, 16, v54\n"
            "v_and_b32 v53, 0xffff0000, v54\n"
            "v_lshlrev_b32 v54, 16, v55\n"
            "v_and_b32 v55, 0xffff0000, v55\n"
            "v_fma_f32 v52, v52, s96, v244\n"
            "v_fma_f32 v53, v53, s96, v245\n"
            "v_fma_f32 v54, v54, s96, v246\n"
            "v_fma_f32 v55, v55, s96, v247\n"
            "v_lshlrev_b32 v56, 16, v58\n"
            "v_and_b32 v57, 0xffff0000, v58\n"
            "v_lshlrev_b32 v58, 16, v59\n"
            "v_and_b32 v59, 0xffff0000, v59\n"
            "v_fma_f32 v56, v56, s96, v248\n"
            "v_fma_f32 v57, v57, s96, v249\n"
            "v_fma_f32 v58, v58, s96, v250\n"
            "v_fma_f32 v59, v59, s96, v251\n"
            "v_lshlrev_b32 v60, 16, v62\n"
            "v_and_b32 v61, 0xffff0000, v62\n"
            "v_lshlrev_b32 v62, 16, v63\n"
            "v_and_b32 v63, 0xffff0000, v63\n"
            "v_fma_f32 v60, v60, s96, v252\n"
            "v_fma_f32 v61, v61, s96, v253\n"
            "v_fma_f32 v62, v62, s96, v254\n"
            "v_fma_f32 v63, v63, s96, v255\n"
            "s_nop 1\n"
            "v_mfma_f32_32x32x16_bf16 v[0:15], v[96:99], v[80:83], v[0:15]\n"
            "ds_read_b128 v[124:127], v130 offset:64\n"
            "v_exp_f32 v16, v16\n"
            "v_exp_f32 v17, v17\n"
            "v_exp_f32 v18, v18\n"
            "v_mfma_f32_32x32x8_bf16 v[0:15], v[100:101], v[88:89], v[0:15]\n"
            "v_exp_f32 v19, v19\n"
            "v_exp_f32 v20, v20\n"
            "v_exp_f32 v21, v21\n"
            "v_mfma_f32_32x32x16_bf16 v[32:47], v[84:87], v[92:95], v[32:47]\n"
            "v_exp_f32 v22, v22\n"
            "v_exp_f32 v23, v23\n"
            "s_nop 5\n"
            "v_exp_f32 v0, v0\n"
            "v_mfma_f32_32x32x16_bf16 v[48:63], v[96:99], v[92:95], v[48:63]\n"
            "v_exp_f32 v1, v1\n"
            "v_exp_f32 v2, v2\n"
            "s_setprio 1\n"
            "v_exp_f32 v3, v3\n"
            "v_mfma_f32_32x32x8_bf16 v[32:47], v[90:91], v[102:103], v[32:47]\n"
            "v_exp_f32 v4, v4\n"
            "v_exp_f32 v5, v5\n"
            "v_exp_f32 v6, v6\n"
            "v_mfma_f32_32x32x8_bf16 v[48:63], v[100:101], v[102:103], v[48:63]\n"
            "s_branch LM2_%=_j\n"
            "LM4_%=:\n"
            "s_sub_u32 s96, s56, s57\n"
            "v_and_b32 v60, 0x1f0, v128\n"
            "s_lshl_b32 s96, s96, 9\n"
            "v_lshrrev_b32 v61, 6, v128\n"
            "s_sub_u32 s96, s54, s96\n"
            "v_and_b32 v61, 8, v61\n"
            "v_lshl_add_u32 v61, v60, 3, v61\n"
            "v_lshrrev_b32 v60, 1, v60\n"
            "v_and_b32 v60, 0x30, v60\n"
            "v_add3_u32 v56, v61, v60, s96\n"
            "v_xor_b32 v57, 16, v56\n"
            "v_xor_b32 v58, 32, v56\n"
            "v_xor_b32 v59, 48, v56\n"
            "s_mov_b32 s96, 0x3fb8aa3b\n"
            "ds_read_b64 v[2:3], v56 offset:64\n"
            "ds_read_b64 v[6:7], v57 offset:64\n"
            "ds_read_b64 v[10:11], v58 offset:64\n"
            "ds_read_b64 v[14:15], v59 offset:64\n"
            "ds_read_b64 v[34:35], v56 offset:4096\n"
            "ds_read_b64 v[38:39], v57 offset:4096\n"
            "ds_read_b64 v[42:43], v58 offset:4096\n"
            "ds_read_b64 v[46:47], v59 offset:4096\n"
            "ds_read_b64 v[48:49], v56\n"
            "ds_read_b64 v[50:51], v57\n"
            "ds_read_b64 v[52:53], v58\n"
            "ds_read_b64 v[54:55], v59\n"
            "s_waitcnt lgkmcnt(8)\n"
            "v_lshlrev_b32 v0, 16, v2\n"
            "v_and_b32 v1, 0xffff0000, v2\n"
            "v_lshlrev_b32 v2, 16, v3\n"
            "v_and_b32 v3, 0xffff0000, v3\n"
            "v_fma_f32 v0, v0, s96, v208\n"
            "v_fma_f32 v1, v1, s96, v209\n"
            "v_fma_f32 v2, v2, s96, v210\n"
            "v_fma_f32 v3, v3, s96, v211\n"
            "v_lshlrev_b32 v4, 16, v6\n"
            "v_and_b32 v5, 0xffff0000, v6\n"
            "v_lshlrev_b32 v6, 16, v7\n"
            "v_and_b32 v7, 0xffff0000, v7\n"
            "v_fma_f32 v4, v4, s96, v212\n"
            "v_fma_f32 v5, v5, s96, v213\n"
            "v_fma_f32 v6, v6, s96, v214\n"
            "v_fma_f32 v7, v7, s96, v215\n"
            "v_lshlrev_b32 v8, 16, v10\n"
            "v_and_b32 v9, 0xffff0000, v10\n"
            "v_lshlrev_b32 v10, 16, v11\n"
            "v_and_b32 v11, 0xffff0000, v11\n"
            "v_fma_f32 v8, v8, s96, v216\n"
            "v_fma_f32 v9, v9, s96, v217\n"
            "v_fma_f32 v10, v10, s96, v218\n"
            "v_fma_f32 v11, v11, s96, v219\n"
            "v_lshlrev_b32 v12, 16, v14\n"
            "v_and_b32 v13, 0xffff0000, v14\n"
            "v_lshlrev_b32 v14, 16, v15\n"
            "v_and_b32 v15, 0xffff0000, v15\n"
            "v_fma_f32 v12, v12, s96, v220\n"
            "v_fma_f32 v13, v13, s96, v221\n"
            "v_fma_f32 v14, v14, s96, v222\n"
            "v_fma_f32 v15, v15, s96, v223\n"
            "s_waitcnt lgkmcnt(0)\n"
            "v_lshlrev_b32 v60, 16, v48\n"
            "v_fmac_f32 v16, s96, v60\n"
            "v_and_b32 v61, 0xffff0000, v48\n"
            "v_fmac_f32 v17, s96, v61\n"
            "v_lshlrev_b32 v62, 16, v49\n"
            "v_fmac_f32 v18, s96, v62\n"
            "v_and_b32 v63, 0xffff0000, v49\n"
            "v_fmac_f32 v19, s96, v63\n"
            "v_lshlrev_b32 v60, 16, v50\n"
            "v_fmac_f32 v20, s96, v60\n"
            "v_and_b32 v61, 0xffff0000, v50\n"
            "v_fmac_f32 v21, s96, v61\n"
            "v_lshlrev_b32 v62, 16, v51\n"
            "v_fmac_f32 v22, s96, v62\n"
            "v_and_b32 v63, 0xffff0000, v51\n"
            "v_fmac_f32 v23, s96, v63\n"
            "v_lshlrev_b32 v60, 16, v52\n"
            "v_fmac_f32 v24, s96, v60\n"
            "v_and_b32 v61, 0xffff0000, v52\n"
            "v_fmac_f32 v25, s96, v61\n"
            "v_lshlrev_b32 v62, 16, v53\n"
            "v_fmac_f32 v26, s96, v62\n"
            "v_and_b32 v63, 0xffff0000, v53\n"
            "v_fmac_f32 v27, s96, v63\n"
            "v_lshlrev_b32 v60, 16, v54\n"
            "v_fmac_f32 v28, s96, v60\n"
            "v_and_b32 v61, 0xffff0000, v54\n"
            "v_fmac_f32 v29, s96, v61\n"
            "v_lshlrev_b32 v62, 16, v55\n"
            "v_fmac_f32 v30, s96, v62\n"
            "v_and_b32 v63, 0xffff0000, v55\n"
            "v_fmac_f32 v31, s96, v63\n"
            "ds_read_b64 v[50:51], v56 offset:4160\n"
            "ds_read_b64 v[54:55], v57 offset:4160\n"
            "ds_read_b64 v[62:63], v59 offset:4160\n"
            "ds_read_b64 v[58:59], v58 offset:4160\n"
            "v_lshlrev_b32 v32, 16, v34\n"
            "v_and_b32 v33, 0xffff0000, v34\n"
            "v_lshlrev_b32 v34, 16, v35\n"
            "v_and_b32 v35, 0xffff0000, v35\n"
            "v_fma_f32 v32, v32, s96, v224\n"
            "v_fma_f32 v33, v33, s96, v225\n"
            "v_fma_f32 v34, v34, s96, v226\n"
            "v_fma_f32 v35, v35, s96, v227\n"
            "v_lshlrev_b32 v36, 16, v38\n"
            "v_and_b32 v37, 0xffff0000, v38\n"
            "v_lshlrev_b32 v38, 16, v39\n"
            "v_and_b32 v39, 0xffff0000, v39\n"
            "v_fma_f32 v36, v36, s96, v228\n"
            "v_fma_f32 v37, v37, s96, v229\n"
            "v_fma_f32 v38, v38, s96, v230\n"
            "v_fma_f32 v39, v39, s96, v231\n"
            "v_lshlrev_b32 v40, 16, v42\n"
            "v_and_b32 v41, 0xffff0000, v42\n"
            "v_lshlrev_b32 v42, 16, v43\n"
            "v_and_b32 v43, 0xffff0000, v43\n"
            "v_fma_f32 v40, v40, s96, v232\n"
            "v_fma_f32 v41, v41, s96, v233\n"
            "v_fma_f32 v42, v42, s96, v234\n"
            "v_fma_f32 v43, v43, s96, v235\n"
            "v_lshlrev_b32 v44, 16, v46\n"
            "v_and_b32 v45, 0xffff0000, v46\n"
            "v_lshlrev_b32 v46, 16, v47\n"
            "v_and_b32 v47, 0xffff0000, v47\n"
            "v_fma_f32 v44, v44, s96, v236\n"
            "v_fma_f32 v45, v45, s96, v237\n"
            "v_fma_f32 v46, v46, s96, v238\n"
            "v_fma_f32 v47, v47, s96, v239\n"
            "s_waitcnt lgkmcnt(0)\n"
            "v_lshlrev_b32 v48, 16, v50\n"
            "v_and_b32 v49, 0xffff0000, v50\n"
            "v_lshlrev_b32 v50, 16, v51\n"
            "v_and_b32 v51, 0xffff0000, v51\n"
            "v_fma_f32 v48, v48, s96, v240\n"
            "v_fma_f32 v49, v49, s96, v241\n"
            "v_fma_f32 v50, v50, s96, v242\n"
            "v_fma_f32 v51, v51, s96, v243\n"
            "v_lshlrev_b32 v52, 16, v54\n"
            "v_and_b32 v53, 0xffff0000, v54\n"
            "v_lshlrev_b32 v54, 16, v55\n"
            "v_and_b32 v55, 0xffff0000, v55\n"
            "v_fma_f32 v52, v52, s96, v244\n"
            "v_fma_f32 v53, v53, s96, v245\n"
            "v_fma_f32 v54, v54, s96, v246\n"
            "v_fma_f32 v55, v55, s96, v247\n"
            "v_lshlrev_b32 v56, 16, v58\n"
            "v_and_b32 v57, 0xffff0000, v58\n"
            "v_lshlrev_b32 v58, 16, v59\n"
            "v_and_b32 v59, 0xffff0000, v59\n"
            "v_fma_f32 v56, v56, s96, v248\n"
            "v_fma_f32 v57, v57, s96, v249\n"
            "v_fma_f32 v58, v58, s96, v250\n"
            "v_fma_f32 v59, v59, s96, v251\n"
            "v_lshlrev_b32 v60, 16, v62\n"
            "v_and_b32 v61, 0xffff0000, v62\n"
            "v_lshlrev_b32 v62, 16, v63\n"
            "v_and_b32 v63, 0xffff0000, v63\n"
            "v_fma_f32 v60, v60, s96, v252\n"
            "v_fma_f32 v61, v61, s96, v253\n"
            "v_fma_f32 v62, v62, s96, v254\n"
            "v_fma_f32 v63, v63, s96, v255\n"
            "s_nop 1\n"
            "v_mfma_f32_32x32x16_bf16 v[0:15], v[96:99], v[80:83], v[0:15]\n"
            "ds_read_b128 v[124:127], v130 offset:64\n"
            "v_exp_f32 v16, v16\n"
            "v_exp_f32 v17, v17\n"
            "v_exp_f32 v18, v18\n"
            "v_mfma_f32_32x32x8_bf16 v[0:15], v[100:101], v[88:89], v[0:15]\n"
            "v_exp_f32 v19, v19\n"
            "v_exp_f32 v20, v20\n"
            "v_exp_f32 v21, v21\n"
            "v_mfma_f32_32x32x16_bf16 v[32:47], v[84:87], v[92:95], v[32:47]\n"
            "v_exp_f32 v22, v22\n"
            "v_exp_f32 v23, v23\n"
            "s_nop 5\n"
            "v_exp_f32 v0, v0\n"
            "v_mfma_f32_32x32x16_bf16 v[48:63], v[96:99], v[92:95], v[48:63]\n"
            "v_exp_f32 v1, v1\n"
            "v_exp_f32 v2, v2\n"
            "v_exp_f32 v3, v3\n"
            "s_setprio 1\n"
            "v_mfma_f32_32x32x8_bf16 v[32:47], v[90:91], v[102:103], v[32:47]\n"
            "v_exp_f32 v4, v4\n"
            "v_exp_f32 v5, v5\n"
            "v_exp_f32 v6, v6\n"
            "v_mfma_f32_32x32x8_bf16 v[48:63], v[100:101], v[102:103], v[48:63]\n"
            "s_branch LM4_%=_j\n"
            "LX5_%=:\n"
            "s_nop 15\n"
            : "={s97}"(bad), "+{s[48:63]}"(sp), "+{v[120:127]}"(ad0), "+{v[128:131]}"(ad1), "+{v[32:47]}"(su), "+{s93}"(tbl), "+{s94}"(wop),
              "={v[132:147]}"(wr0), "={v[148:163]}"(wr1), "={v[164:179]}"(wr2), "={v[180:187]}"(wr3), "={v[188:191]}"(wr4),
              "={v[192:207]}"(bias[0]), "={v[208:223]}"(bias[1]), "={v[224:239]}"(bias[2]), "={v[240:255]}"(bias[3])
            : "{s[36:39]}"(rsX), "{s[40:43]}"(rsM), "{s[44:47]}"(rsO), "{s[64:79]}"(tb0), "{s[80:83]}"(tb1), "{s[88:91]}"(rsWo)
            : "v0", "v1", "v2", "v3", "v4", "v5", "v6", "v7", "v8", "v9", "v10", "v11", "v12", "v13", "v14", "v15", "v16", "v17", "v18", "v19",
              "v20", "v21", "v22", "v23", "v24", "v25", "v26", "v27", "v28", "v29", "v30", "v31", "v48", "v49", "v50", "v51", "v52", "v53", "v54", "v55",
              "v56", "v57", "v58", "v59", "v60", "v61", "v62", "v63", "v64", "v65", "v66", "v67", "v68", "v69", "v70", "v71", "v72", "v73", "v74", "v75",
              "v76", "v77", "v78", "v79", "v80", "v81", "v82", "v83", "v84", "v85", "v86", "v87", "v88", "v89", "v90", "v91", "v92", "v93", "v94", "v95",
              "v96", "v97", "v98", "v99", "v100", "v101", "v102", "v103", "v104", "v105", "v106", "v107", "v108", "v109", "v110", "v111", "v112", "v113", "v114", "v115",
              "v116", "v117", "v118", "v119", "s84", "s85", "s86", "s87", "s92", "s95", "s96", "vcc", "scc", "memory");
        hd2 = (sp[8] - sp[9]) >> 2; gp2 = sp[10];
    }

    if (ad1[0] == 0) *(LDS unsigned*)(smem + L_BAD + 16 * gp2 + 4 * hd2) = bad;
    asm volatile("s_waitcnt lgkmcnt(0)\n\ts_barrier" ::: "memory");
    {
        const u32x4 a = ld128(smem + L_BAD) | ld128(smem + L_BAD + 16);
        bad = __builtin_amdgcn_readfirstlane(a.x | a.y | a.z | a.w);
    }
    if (bad == 0) return;
    {
    const int head = hd2, srow = (blockIdx.x & 31) + 32 * gp2, scol = (blockIdx.x >> 5) ^ gp2;
#define S4(v, k) u32x4{v[k], v[k + 1], v[k + 2], v[k + 3]}
    const u32x4 WA[6] = {S4(wr0, 0), S4(wr0, 4), S4(wr0, 8), S4(wr0, 12), S4(wr1, 0), S4(wr1, 4)}, WV[6] = {S4(wr1, 8), S4(wr1, 12), S4(wr2, 0), S4(wr2, 4), S4(wr2, 8), S4(wr2, 12)};
    const u32x4 WB[3] = {S4(wr3, 0), S4(wr3, 4), wr4};
    const int lx0 = gp2 ? L_B + 32768 : L_A + 8192, lm0 = gp2 ? L_B + 57344 : L_A, lo = gp2 ? L_B + 8192 : L_A + 45056;
    auto win_y = [&](int n) { return n ? (srow + 9 * n) & 63 : win0_y(srow, scol); };
    auto win_x = [&](int n) { return n ? 8 * (NR - 1 - n) + scol : win0_x(srow, scol); };
    auto x_off = [&](int n) { return (unsigned)(((8 * win_y(n) + 4 + 2 * head) & 511) * ROWB + (win_x(n) == 63 ? 0 : (8 * win_x(n) + 4) * 192)); };
    auto m_off = [&](int n) { return (unsigned)((win_y(n) * 64 + win_x(n)) * 8192 + head * 2048); };
#pragma unroll 1
    for (int n = 0; n < NR; n++) {
        if (!((bad >> (NR - 1 - n)) & 1)) continue;
        const int ln = ad1[0] >> 4;
        const bool ed = win_x(n) == 63;
#pragma unroll
        for (int i = 0; i < 3; i++) {
            const int p = 64 * i + ln, r = p / 12;
            const unsigned v = (r >> 3) * ROWB + (r & 7) * 192 + 16 * ((p - 12 * r) ^ swz(r >> 2)) + (!ed ? 0 : (r & 7) >= 4 ? -768 : 508 * 192);
            DMA(rsX, v, x_off(n), lx0 + head * 3072 + 1024 * i, "");
        }
        const unsigned mv = ad1[0] ^ (16 * (ln >> 4));
        DMA(rsM, mv, m_off(n), lm0 + head * 2048, ""); DMA(rsM, mv, m_off(n) + 1024, lm0 + head * 2048 + 1024, "");
        WAIT_BAR(0);
        const int gr = swz((ln & 31) >> 2);
        const unsigned yaE = (ln & 31) * 192 + 16 * ((ln >> 5) ^ gr), yaO = (ln & 31) * 192 + 16 * ((2 + (ln >> 5)) ^ gr), ya16 = (ln & 15) * 192 + 16 * ((ln >> 4) ^ swz((ln & 15) >> 2));
        const unsigned ones = (ln & 31) < 24 ? 0u : 0x3f803f80u;
        u32x4 Q0[2], K0[2], Vf[4]; u32x2 Q1[2], K1[2];
#pragma unroll
        for (int T = 0; T < 2; T++) {
            f32x16 aa = {}, av = {}; f32x4 b0 = {}, b1 = {};
#pragma unroll
            for (int s = 0; s < 6; s++) {
                const u32x4 xf = ld128(smem + lx0 + ((s & 1) ? yaO : yaE) + 64 * (s >> 1) + 6144 * T);
                aa = mfma32(WA[s], xf, aa); av = mfma32(xf, WV[s], av);
            }
#pragma unroll
            for (int s = 0; s < 3; s++) {
                b0 = mfma16(WB[s], ld128(smem + lx0 + ya16 + 64 * s + 6144 * T), b0);
                b1 = mfma16(WB[s], ld128(smem + lx0 + ya16 + 64 * s + 6144 * T + 3072), b1);
            }
            Q0[T] = u32x4{pk(aa[0], aa[1]), pk(aa[2], aa[3]), pk(aa[4], aa[5]), pk(aa[6], aa[7])};
            K0[T] = u32x4{pk(aa[8], aa[9]), pk(aa[10], aa[11]), pk(aa[12], aa[13]), pk(aa[14], aa[15])};
            const auto r0 = __builtin_amdgcn_permlane16_swap(pk(b0[0], b0[1]), pk(b1[0], b1[1]), false, false);
            const auto r1 = __builtin_amdgcn_permlane16_swap(pk(b0[2], b0[3]), pk(b1[2], b1[3]), false, false);
            Q1[T] = u32x2{r0[0], r1[0]}; K1[T] = u32x2{r0[1], r1[1]};
            Vf[2 * T]     = u32x4{pk(av[0], av[1]) | ones, pk(av[2], av[3]) | ones, pk(av[4], av[5]) | ones, pk(av[6], av[7]) | ones};
            Vf[2 * T + 1] = u32x4{pk(av[8], av[9]) | ones, pk(av[10], av[11]) | ones, pk(av[12], av[13]) | ones, pk(av[14], av[15]) | ones};
            PIN(Q0[T]); PIN(Q1[T]); PIN(K0[T]); PIN(K1[T]); PIN(Vf[2 * T]); PIN(Vf[2 * T + 1]); SBAR();
        }
#pragma unroll
        for (int U = 0; U < 2; U++) {
            f32x16 s0 = mfma32(K0[0], Q0[U], bias[2 * U]), s1 = mfma32(K0[1], Q0[U], bias[2 * U + 1]);
            s0 = mfma32h(K1[0], Q1[U], s0); s1 = mfma32h(K1[1], Q1[U], s1);
            const int row = 32 * U + (ln & 31), sx = (row >> 1) & 3;
#pragma unroll
            for (int k = 0; k < 4; k++) {
                LDS const char* mp = smem + lm0 + row * 128 + 16 * (k ^ sx) + 8 * (ln >> 5);
                const u32x2 a = ld64(mp), b = ld64(mp + 64);
                s0[4 * k]     = __builtin_fmaf(__uint_as_float(a.x << 16), log2e, s0[4 * k]);     s0[4 * k + 1] = __builtin_fmaf(__uint_as_float(a.x & 0xffff0000u), log2e, s0[4 * k + 1]);
                s0[4 * k + 2] = __builtin_fmaf(__uint_as_float(a.y << 16), log2e, s0[4 * k + 2]); s0[4 * k + 3] = __builtin_fmaf(__uint_as_float(a.y & 0xffff0000u), log2e, s0[4 * k + 3]);
                s1[4 * k]     = __builtin_fmaf(__uint_as_float(b.x << 16), log2e, s1[4 * k]);     s1[4 * k + 1] = __builtin_fmaf(__uint_as_float(b.x & 0xffff0000u), log2e, s1[4 * k + 1]);
                s1[4 * k + 2] = __builtin_fmaf(__uint_as_float(b.y << 16), log2e, s1[4 * k + 2]); s1[4 * k + 3] = __builtin_fmaf(__uint_as_float(b.y & 0xffff0000u), log2e, s1[4 * k + 3]);
            }
            float m = mx2(s0[0], s1[0]);
#pragma unroll
            for (int r = 1; r < 16; r++) m = mx2(mx2(m, s0[r]), s1[r]);
            { const auto rr = __builtin_amdgcn_permlane32_swap(__float_as_uint(m), __float_as_uint(m), false, false); m = mx2(__uint_as_float(rr[0]), __uint_as_float(rr[1])); }
#pragma unroll
            for (int r = 0; r < 16; r++) { s0[r] = __builtin_amdgcn_exp2f(s0[r] - m); s1[r] = __builtin_amdgcn_exp2f(s1[r] - m); }
            f32x16 o = {};
            o = mfma32(Vf[0], u32x4{pk(s0[0], s0[1]), pk(s0[2], s0[3]), pk(s0[4], s0[5]), pk(s0[6], s0[7])}, o);
            o = mfma32(Vf[1], u32x4{pk(s0[8], s0[9]), pk(s0[10], s0[11]), pk(s0[12], s0[13]), pk(s0[14], s0[15])}, o);
            o = mfma32(Vf[2], u32x4{pk(s1[0], s1[1]), pk(s1[2], s1[3]), pk(s1[4], s1[5]), pk(s1[6], s1[7])}, o);
            o = mfma32(Vf[3], u32x4{pk(s1[8], s1[9]), pk(s1[10], s1[11]), pk(s1[12], s1[13]), pk(s1[14], s1[15])}, o);
            const float inv = __builtin_amdgcn_rcpf(o[12]);
#pragma unroll
            for (int p = 0; p < 3; p++)
                *(LDS u32x2*)(smem + ad0[3] + 6144 * U + 64 * p) = u32x2{pk(o[4 * p] * inv, o[4 * p + 1] * inv), pk(o[4 * p + 2] * inv, o[4 * p + 3] * inv)};
            SBAR();
        }
        WAIT_BAR(0);
        f32x4 oa[6] = {};
#pragma unroll
        for (int s = 0; s < 3; s++) {
            const u32x4 of = ld128(smem + ad1[2] + 64 * s);
#pragma unroll
            for (int u = 0; u < 6; u++) oa[u] = mfma16(of, ld128(smem + L_WO + (6 * s + u) * 1024 + ad1[0]), oa[u]);
        }
#pragma unroll
        for (int r = 0; r < 4; r++)
            __builtin_amdgcn_raw_buffer_store_b96(u32x3{pk(oa[0][r], oa[1][r]), pk(oa[2][r], oa[3][r]), pk(oa[4][r], oa[5][r])}, rsO, ad1[1],
                                                  x_off(n) + (r >> 1) * ROWB + ((r & 1) ? (ed ? 0 : 768) : (ed ? 508 * 192 : 0)), 0);
        WAIT_BAR(0);
    }
    }
}


extern "C" void kernel_launch(void* const* d_in, const int* in_sizes, int n_in, void* d_out, int out_size, void* d_ws, size_t ws_size, hipStream_t stream) {
    swin_win_attn_kernel<<<256, 512, 0, stream>>>((const char*)d_in[0], (const char*)d_in[1], (const char*)d_in[2], (const unsigned short*)d_in[3],
                                                   (const int*)d_in[4], (const char*)d_in[5], (char*)d_out
    );
}
```
